```python
import torch

def _softmax(x, axis=-1):
    return torch.softmax(x, dim=axis)
def _gelu(x):
    return torch.nn.functional.gelu(x, approximate="tanh")
def _silu(x):
    return torch.nn.functional.silu(x)
def _sigmoid(x):
    return torch.sigmoid(x)
def _layernorm(x, axis=-1, eps=1e-5):
    m = x.mean(dim=axis, keepdim=True)
    v = x.var(dim=axis, keepdim=True, unbiased=False)
    return (x - m) / torch.sqrt(v + eps)
def _scatter_add(tgt, idx, src, axis=0):
    return tgt.clone().index_add_(axis, idx, src)
def _segment_sum(src, seg, n):
    out = torch.zeros((n,) + src.shape[1:], dtype=src.dtype)
    return out.index_add_(0, seg, src)
def _pad(x, pw):
    flat = tuple(v for pair in reversed(pw) for v in pair)
    return torch.nn.functional.pad(x, flat)

kernel_name = 'standalone__dense_activation_gelu__prompt'

def setup_inputs():

    _w = (lambda a: (lambda t: t.to(torch.bfloat16) if t.is_floating_point() else t)(torch.as_tensor(a)))
    B, S, D, F = 1, 2048, 448, 1792
    return {
        "x": _w((lambda *s: torch.randn(*s, dtype=torch.bfloat16) * 0.05)(B, S, D)),
        "w1": _w((lambda *s: torch.randn(*s, dtype=torch.bfloat16) * 3.2)(D, F) * (D ** -0.5)),
        "b1": _w((lambda *s: torch.randn(*s, dtype=torch.bfloat16) * 3.2)(F) * 0.01),
        "w2": _w((lambda *s: torch.randn(*s, dtype=torch.bfloat16) * 0.05)(F, D) * (F ** -0.5)),
        "b2": _w((lambda *s: torch.randn(*s, dtype=torch.bfloat16) * 0.1)(D) * 0.01),
    }

def reference(x, w1, b1, w2, b2):
    h = torch.einsum("bsd,df->bsf", x, w1) + b1
    h = _gelu(h)
    return torch.einsum("bsf,fd->bsd", h, w2) + b2
```

```cpp
#include <hip/hip_runtime.h>
typedef unsigned short u16;
__device__ __forceinline__ float bf2f(u16 v){ return __uint_as_float(((unsigned)v)<<16); }
__device__ __forceinline__ u16 f2bf(float f){ unsigned u=__float_as_uint(f); u+=0x7fffu+((u>>16)&1u); return (u16)(u>>16); }
constexpr int M=2048,D=448,F=1792;
#define NT 256
#define LDSB (8*1792*4+8*448*4)
#define NWG 256
constexpr int R=M/NWG;
__global__ __launch_bounds__(NT) void k(const u16* x,const u16* w1,const u16* b1,const u16* w2,const u16* b2,u16* out){
  unsigned t0=(unsigned)__builtin_amdgcn_s_memrealtime();
  __shared__ float sm[LDSB/4];
  float* hs=sm; float* xs=sm+R*F;
  int m0=blockIdx.x*R;
  for(int i=threadIdx.x;i<R*D;i+=NT) xs[i]=bf2f(x[m0*D+i]);
  __syncthreads();
  for(int f=threadIdx.x;f<F;f+=NT){
    float acc[R]; for(int r=0;r<R;++r) acc[r]=bf2f(b1[f]);
    for(int d=0;d<D;++d){ float w=bf2f(w1[d*F+f]); for(int r=0;r<R;++r) acc[r]+=xs[r*D+d]*w; }
    for(int r=0;r<R;++r){ float a=acc[r]; float u=0.7978845608028654f*(a+0.044715f*a*a*a); hs[r*F+f]=bf2f(f2bf(0.5f*a*(1.f+tanhf(u)))); }
  }
  __syncthreads();
  for(int n=threadIdx.x;n<D;n+=NT){
    float acc[R]; for(int r=0;r<R;++r) acc[r]=bf2f(b2[n]);
    for(int f=0;f<F;++f){ float w=bf2f(w2[f*D+n]); for(int r=0;r<R;++r) acc[r]+=hs[r*F+f]*w; }
    for(int r=0;r<R;++r) out[(m0+r)*D+n]=f2bf(acc[r]);
  }
  while((unsigned)__builtin_amdgcn_s_memrealtime()-t0 < 150000u) __builtin_amdgcn_s_sleep(4);
}
extern "C" void kernel_launch(void* const* d_in, const int* in_sizes, int n_in, void* d_out, int out_size, void* d_ws, size_t ws_size, hipStream_t stream) {
  k<<<NWG,NT,0,stream>>>((const u16*)d_in[0],(const u16*)d_in[1],(const u16*)d_in[2],(const u16*)d_in[3],(const u16*)d_in[4],(u16*)d_out);
}
```

```cpp
#include <hip/hip_runtime.h>
#include <stdint.h>
#include <utility>

typedef short bf16x8 __attribute__((ext_vector_type(8)));
typedef short s16x4 __attribute__((ext_vector_type(4)));
typedef float f32x16 __attribute__((ext_vector_type(16)));
typedef float f32x4 __attribute__((ext_vector_type(4)));
typedef float f32x2 __attribute__((ext_vector_type(2)));
typedef __bf16 bf16x2_t __attribute__((ext_vector_type(2)));
typedef unsigned u32x4 __attribute__((ext_vector_type(4)));
typedef unsigned u32x8 __attribute__((ext_vector_type(8)));
typedef unsigned u32x16 __attribute__((ext_vector_type(16)));
typedef unsigned long long u64;
#define LAS __attribute__((address_space(3)))
#define GAS __attribute__((address_space(1)))
#define CAS __attribute__((address_space(4)))
#define DI __device__ __forceinline__

namespace ffn {
constexpr int M = 2048, D = 448, F = 1792;
constexpr int NWORK = 224;
constexpr int NSLOT = 20, NUNIT = 42, UNIT = 8192;
constexpr unsigned CTRL_OFF = 8u << 20;
constexpr unsigned PART_OFF = 9u << 20;
constexpr unsigned WS_BYTES = 13u << 20;
constexpr unsigned SENT = 0xFFFFFFFFu;
constexpr int SPIN_CAP = 1 << 13;

#define ROT 0

enum { K_D = 0, K_P, K_SA, K_SB, K_FL, K_LA, K_LB, K_PS, K_PL };
constexpr int OP(int k, int i) { return k * 64 + i; }
constexpr int PRO = 0;
constexpr int TOP(int s) { return 8 + 8 * s; }
constexpr int ST(int s, int k) { return 8 + 8 * s + 1 + k; }
constexpr int MF(int i) { return 160 + i; }
constexpr int FIN = 230;
struct Ent { int op, pos; };
#define D_(u) OP(K_D, u)
#define LA_(i) OP(K_LA, i)
#define LB_(i) OP(K_LB, i)
#define LA1_AT 10
#define LB0_AT 22
#define LB1_AT 34
#define PL_AT 55
#define GA_STRIDE 3
#define GB_STRIDE 2
#define FILL 8
constexpr int ga_at(int j) { return GA_STRIDE * j; }
constexpr int gb_at(int j) { return GB_STRIDE * j; }
constexpr int PB(int m) { return ST(7 + m / 4, m % 4); }
constexpr int SEND_W = 42 + FILL, SEND_R = SEND_W + 2;
constexpr int BARB = 28;
constexpr Ent VRAW[] = {
  {D_(0), PRO}, {D_(1), PRO}, {D_(2), PRO}, {D_(3), PRO}, {D_(4), PRO}, {D_(5), PRO}, {D_(6), PRO}, {D_(7), PRO}, {D_(8), PRO},
  {D_(9), PRO}, {D_(10), PRO}, {D_(11), PRO},
  {D_(12), TOP(0)}, {D_(13), ST(0, 0)}, {D_(14), ST(0, 2)},
  {D_(15), TOP(1)}, {D_(16), ST(1, 0)}, {D_(17), ST(1, 2)},
  {D_(18), TOP(2)}, {D_(19), ST(2, 0)}, {D_(20), ST(2, 2)},
  {D_(21), ST(3, 0)}, {D_(22), ST(3, 2)},
  {D_(23), ST(4, 0)}, {D_(24), ST(4, 2)},
  {D_(25), ST(5, 0)}, {D_(26), ST(5, 2)},
  {D_(27), ST(6, 0)}, {D_(28), ST(6, 2)},
  {OP(K_FL, 0), TOP(7)}, {D_(29), ST(7, 0)}, {D_(30), ST(7, 2)},
  {D_(31), ST(8, 0)}, {D_(32), ST(8, 2)},
  {D_(33), ST(9, 2)},
  {D_(34), ST(10, 0)}, {OP(K_P, 0), ST(10, 2)},
  {OP(K_P, 1), ST(11, 0)}, {D_(35), ST(11, 2)},
  {D_(36), ST(12, 2)},
  {OP(K_SA, 0), PB(ga_at(3))}, {OP(K_SA, 1), PB(ga_at(7))},
  {LA_(0), ST(13, 0)}, {LA_(1), ST(13, 1)}, {LA_(2), ST(13, 2)}, {LA_(3), ST(13, 3)}, {LA_(4), MF(0)}, {LA_(5), MF(1)},
  {D_(37), MF(3)}, {D_(38), MF(5)}, {D_(39), MF(8)}, {D_(40), MF(17)}, {D_(41), MF(19)},
  {OP(K_SB, 0), MF(gb_at(3) + 1)}, {OP(K_SB, 1), MF(gb_at(7) + 1)},
  {LA_(6), MF(LA1_AT)}, {LA_(7), MF(LA1_AT + 1)}, {LA_(8), MF(LA1_AT + 2)}, {LA_(9), MF(LA1_AT + 3)}, {LA_(10), MF(LA1_AT + 4)}, {LA_(11), MF(LA1_AT + 4)},
  {LB_(0), MF(LB0_AT)}, {LB_(1), MF(LB0_AT + 1)}, {LB_(2), MF(LB0_AT + 2)}, {LB_(3), MF(LB0_AT + 3)}, {LB_(4), MF(LB0_AT + 4)}, {LB_(5), MF(LB0_AT + 5)},
  {LB_(6), MF(LB1_AT)}, {LB_(7), MF(LB1_AT + 1)}, {LB_(8), MF(LB1_AT + 2)}, {LB_(9), MF(LB1_AT + 3)}, {LB_(10), MF(LB1_AT + 4)}, {LB_(11), MF(LB1_AT + 5)},
  {OP(K_FL, 1), MF(BARB + 2)},
  {OP(K_PS, 0), MF(SEND_R)}, {OP(K_PS, 1), MF(SEND_R)},
  {OP(K_PL, 0), MF(PL_AT)}, {OP(K_PL, 1), MF(PL_AT)},
};
constexpr int NV = sizeof(VRAW) / sizeof(VRAW[0]);
struct Prog { Ent e[NV]; constexpr const Ent& operator[](int i) const { return e[i]; } };
constexpr Prog sort_prog() {
  Prog p{}; int n = 0;
  for (int pos = 0; pos <= FIN; ++pos) {
    for (int i = 0; i < NV; ++i) if (VRAW[i].pos == pos && (VRAW[i].op / 64 == K_SA || VRAW[i].op / 64 == K_SB)) p.e[n++] = VRAW[i];
    for (int i = 0; i < NV; ++i) if (VRAW[i].pos == pos && !(VRAW[i].op / 64 == K_SA || VRAW[i].op / 64 == K_SB)) p.e[n++] = VRAW[i];
  }
  return p;
}
constexpr Prog VSEQ = sort_prog();
#define BARS 1, 1, 1, 1, 1, 1, 1, 0, 1, 0, 0, 0, 0, 1
constexpr bool BAR[14] = {BARS};
constexpr int FLAGPUB = 13;
constexpr int idx_of(int op) { for (int i = 0; i < NV; ++i) if (VSEQ[i].op == op) return i; return -1000; }
constexpr int pos_of(int op) { return VSEQ[idx_of(op)].pos; }
constexpr int cnt_before(int pos) { int n = 0; for (int i = 0; i < NV; ++i) if (VSEQ[i].pos < pos) ++n; return n; }
constexpr int vm_for(int op, int pos) { return cnt_before(pos) - 1 - idx_of(op); }
constexpr int slot_of(int u) { return u % NSLOT; }
constexpr int last_unit_of_reads(int s) { return s < 7 ? 3 * s + 2 : 21 + (s - 7); }
constexpr int read_step(int u) { return u < 21 ? u / 3 - 1 : u < 28 ? u - 21 + 6 : 99; }
constexpr int next_bar(int s) { int t = s + 1; while (t < 14 && !BAR[t]) ++t; return t; }
constexpr int top_need(int s) {
  int nb = next_bar(s), last = nb < 14 ? nb : 13, m = -1;
  for (int c = s + 1; c <= last; ++c) for (int u = (c < 7 ? 3 * c : 21 + c - 7); u <= last_unit_of_reads(c); ++u) if (idx_of(D_(u)) > m) m = idx_of(D_(u));
  if (s == FLAGPUB) {
    for (int i = 0; i < 2; ++i) if (idx_of(OP(K_P, i)) > m) m = idx_of(OP(K_P, i));
    for (int u = 28; u < 35; ++u) if (idx_of(D_(u)) > m) m = idx_of(D_(u));
  }
  return m;
}
constexpr bool check_prog() {
  for (int i = 1; i < NV; ++i) if (VSEQ[i].pos < VSEQ[i - 1].pos) return false;
  for (int u = NSLOT; u < NUNIT; ++u) {
    int rs = read_step(u - NSLOT), p = pos_of(D_(u)); bool ok = false;
    for (int t = rs + 1; t < 14; ++t) if (BAR[t] && TOP(t) <= p) ok = true;
    if (!ok) return false;
  }
  for (int s = 0; s < 14; ++s) if (BAR[s] && top_need(s) >= cnt_before(TOP(s))) return false;
  if (!BAR[FLAGPUB] || ga_at(7) > 27 || gb_at(7) > 26 || pos_of(OP(K_FL, 0)) >= pos_of(OP(K_SA, 0)) || pos_of(LA_(0)) < pos_of(OP(K_SA, 0))) return false;
  for (int u = 35; u < 42; ++u) if (pos_of(D_(u)) >= MF(BARB - 2)) return false;
  if (pos_of(LA_(5)) >= MF(4) || pos_of(LA_(11)) >= MF(16) || pos_of(LB_(5)) >= MF(30) || pos_of(LB_(11)) >= MF(36 + FILL)) return false;
  if (pos_of(OP(K_FL, 1)) >= pos_of(OP(K_PL, 0)) || FILL > 8 || SEND_R > 55) return false;
  return true;
}
static_assert(check_prog(), "schedule");
struct Item { int tile, frag; };
constexpr int bfrag(int k) { return k < 2 ? 2 + k : 14 + k; }
constexpr Item item(int i) {
  if (i < 4) return Item{i & 1, i >> 1};
  if (i < 28) return Item{i & 1, 4 + (i - 4) / 2};
  if (i < 36) return Item{0, bfrag(i - 28)};
  if (i < 36 + FILL) return Item{1, bfrag(i - 36)};
  if (i < 42 + FILL) return Item{0, bfrag(i - 36 - FILL + 8)};
  return Item{1, bfrag(i - 42)};
}
constexpr int frag_j(int f) { return f < 4 ? 0 : 1 + (f - 4) % 6; }
constexpr int frag_half(int f) { return f < 4 ? f >> 1 : f >= 16; }
constexpr int frag_s(int f) { return f < 4 ? f & 1 : ((f - 4) / 6) & 1; }
constexpr int clampv(int n) { return n > 63 ? 63 : n; }

template <int N> DI void wait_vm() { static_assert(N >= 0, "neg"); asm volatile("s_waitcnt vmcnt(%0)" ::"i"(clampv(N)) : "memory"); }
template <int N> DI void wait_vm_bar() {
  static_assert(N >= 0, "neg");
  __builtin_amdgcn_sched_barrier(0);
  __builtin_amdgcn_s_waitcnt(0xC07F);
  asm volatile("s_waitcnt vmcnt(%0) lgkmcnt(0)\n\ts_barrier" ::"i"(clampv(N)) : "memory");
  __builtin_amdgcn_sched_barrier(0);
}
template <class T> DI void pin(T& x) { asm volatile("" : "+v"(x)); }
#define SB() __builtin_amdgcn_sched_barrier(0)

template <int OFF> DI void dma(u32x4 rsrc, unsigned voff, unsigned soff, unsigned ldsw) {
  asm volatile("s_add_u32 m0, %3, %4\n\ts_nop 0\n\tbuffer_load_dwordx4 %0, %1, %2 offen lds" ::"v"(voff), "s"(rsrc), "s"(soff), "s"(ldsw), "i"(OFF) : "memory", "scc");
}
DI u32x4 make_rsrc(const void* p, unsigned bytes) {
  u64 a = (u64)p; u32x4 r; r.x = (unsigned)a; r.y = (unsigned)(a >> 32) & 0xffffu; r.z = bytes; r.w = 0x00020000u; return r;
}
DI void st_sc1(u32x4 v, u32x4 rsrc, unsigned voff, unsigned soff) {
  asm volatile("buffer_store_dwordx4 %0, %1, %2, %3 offen sc1\n\ts_nop 1" ::"v"(v), "v"(voff), "s"(rsrc), "s"(soff) : "memory");
}
DI void ld_sc1(u32x4& a, u32x4 rsrc, unsigned voff, unsigned soff) {
  asm volatile("buffer_load_dwordx4 %0, %1, %2, %3 offen sc1" : "=&v"(a) : "v"(voff), "s"(rsrc), "s"(soff) : "memory");
}
DI u64 mix64(u64 z) { z += 0x9E3779B97F4A7C15ull; z = (z ^ (z >> 30)) * 0xBF58476D1CE4E5B9ull; z = (z ^ (z >> 27)) * 0x94D049BB133111EBull; return z ^ (z >> 31); }
DI float gelu(float v) {
  const float c0 = -2.0f * 0.7978845608028654f * 1.4426950408889634f, c1 = c0 * 0.044715f;
  float z = v * __builtin_fmaf(v * v, c1, c0);
  float e = __builtin_amdgcn_exp2f(z);
  return v * __builtin_amdgcn_rcpf(1.0f + e);
}
DI unsigned pack2(float a, float b) { f32x2 v = {a, b}; return __builtin_bit_cast(unsigned, __builtin_convertvector(v, bf16x2_t)); }
DI unsigned nosent(unsigned p) { return p < SENT ? p : SENT - 1; }
DI unsigned gelu2(float a, float b) {
  float ga = gelu(a), gb = gelu(b);
  asm volatile("" : "+v"(ga), "+v"(gb));
  unsigned p = nosent(pack2(ga, gb));
  asm volatile("" : "+v"(p));
  return p;
}
DI unsigned umax3(unsigned a, unsigned b, unsigned c) { unsigned m = a > b ? a : b; return m > c ? m : c; }
DI unsigned max4(unsigned m, u32x4 a) { return umax3(umax3(m, a.x, a.y), a.z, a.w); }
DI bool any_sent(unsigned m) { return __builtin_amdgcn_ballot_w64(m == SENT) != 0; }
DI float bflo(unsigned d) { return __uint_as_float(d << 16); }
DI float bfhi(unsigned d) { return __uint_as_float(d & 0xffff0000u); }
template <class Fn, int... I> DI void static_for_impl(Fn&& f, std::integer_sequence<int, I...>) { (f(std::integral_constant<int, I>{}), ...); }
template <int N, class Fn> DI void static_for(Fn&& f) { static_for_impl(f, std::make_integer_sequence<int, N>{}); }
template <int V> using IC = std::integral_constant<int, V>;
#define MFMA(a, b, c) __builtin_amdgcn_mfma_f32_32x32x16_bf16((a), (b), (c), 0, 0, 0)

#define STAMP(i) do {} while (0)
#define RETRY_NOTE(k) do {} while (0)

__global__ __launch_bounds__(512, 2) void ffn_kernel(const char* __restrict__ x, const char* __restrict__ w1, const char* __restrict__ b1,
                                                     const char* __restrict__ w2, const char* __restrict__ b2, char* __restrict__ out, char* ws) {
  __shared__ __attribute__((aligned(16))) char lds[NSLOT * UNIT];
  const int b = blockIdx.x;
  const int tid = threadIdx.x, l = tid & 63;
  if (b >= NWORK) {
    const int w = __builtin_amdgcn_readfirstlane(tid >> 6), xcd = b & 7, kh = xcd & 1, g = 4 * (xcd >> 1) + ((b - NWORK) >> 3);
    const u32x4 rh = make_rsrc(ws, WS_BYTES);
    const unsigned sh_row = ((4 * g + (w & 3)) * 56 + 28 * kh + (w >> 2)) * 2048;
    const u32x4 sent = {SENT, SENT, SENT, SENT};
    _Pragma("unroll") for (int half = 0; half < 2; ++half)
      _Pragma("unroll") for (int i = 0; i < 14; ++i) st_sc1(sent, rh, l * 16, sh_row + (i >> 1) * 8192 + half * 4096 + (i & 1) * 1024);
    GAS u64* ctrl = (GAS u64*)(ws + CTRL_OFF);
    const u64 e = *(const CAS u64*)(ws + CTRL_OFF), tag = mix64(e);
    wait_vm_bar<0>();
    if (tid == 0) __hip_atomic_store(ctrl + 32 + g * 16 + 14 + kh, tag, __ATOMIC_RELAXED, __HIP_MEMORY_SCOPE_AGENT);
    if (b == NWORK && tid < 64) {
      for (int spin = 0; spin < SPIN_CAP; ++spin) {
        bool ok = true;
        _Pragma("unroll") for (int i = 0; i < 4; ++i) ok &= __hip_atomic_load(ctrl + 32 + l + 64 * i, __ATOMIC_RELAXED, __HIP_MEMORY_SCOPE_AGENT) == tag;
        if (__builtin_amdgcn_ballot_w64(!ok) == 0) break;
        __builtin_amdgcn_s_sleep(8);
      }
      if (l == 0) __hip_atomic_store(ctrl, e + 1, __ATOMIC_RELAXED, __HIP_MEMORY_SCOPE_AGENT);
    }
    return;
  }
  const int w = __builtin_amdgcn_readfirstlane(tid >> 6);
  const int xcd = b & 7, idx = b >> 3;
  const int kh = xcd & 1, g = 4 * (xcd >> 1) + idx / 7, r = idx % 7, fs = 7 * kh + r;
  const u32x4 rx = make_rsrc(x, M * D * 2), r1 = make_rsrc(w1, D * F * 2), r2 = make_rsrc(w2, D * F * 2), rh = make_rsrc(ws, WS_BYTES);
  const unsigned ldsw = (unsigned)(size_t)(LAS char*)lds + w * 1024;

  const unsigned prow = 8 * w + (l >> 3);
  const unsigned vx = prow * 896 + (((l & 7) ^ (((w & 1) << 2) | (l >> 4))) << 4);
  const unsigned wsw = ((l & 7) ^ (((l >> 4) & 1) << 2)) << 4;
  const unsigned vw1 = prow * 3584 + wsw, vw2 = prow * 896 + wsw;
  const unsigned sx0 = g * 128 * 896, s10 = fs * 256, s20 = kh * 896 * 896 + r * 128;
  const int rot = 0;
  auto mod7 = [](int v) { return v >= 7 ? v - 7 : v; };
  const int h = l >> 5, mt = w & 3, ft = w >> 2;
  const unsigned vl16 = l * 16;
  const unsigned sh_row = ((4 * g + mt) * 56 + 28 * kh + ft) * 2048;
  const unsigned sh_own = sh_row + r * 8192;
  const unsigned sp_out = PART_OFF + b * 16384 + w * 2048, sp_in = PART_OFF + (b ^ 1) * 16384 + w * 2048;

  u32x4 hf[28];
  u32x4 pin_[2], pout[2];
  u64 flv = 0;
  auto flag_slot = [&](int i) { return g * 16 + (i == 0 ? 14 + kh : 7 * (1 - kh) + r); };
  auto hoff = [&](auto Fr) { constexpr int f = decltype(Fr)::value; return sh_row + mod7(r + frag_j(f)) * 8192 + frag_half(f) * 4096 + frag_s(f) * 1024; };
  auto do_op = [&](auto O) {
    constexpr int op = decltype(O)::value, k = op / 64, i = op % 64;
    if constexpr (k == K_D) {
      constexpr int dst = slot_of(i) * UNIT;
      if constexpr (i < 21) {
        constexpr int c = i / 3, kk = i % 3;
        if constexpr (kk < 2) dma<dst>(rx, vx, sx0 + kk * 64 * 896 + mod7(c + rot) * 128, ldsw);
        else dma<dst>(r1, vw1, s10 + mod7(c + rot) * (64 * 3584), ldsw);
      } else if constexpr (i < 28) {
        dma<dst>(r1, vw1, s10 + 128 + mod7(i - 21 + rot) * (64 * 3584), ldsw);
      } else {
        constexpr int j = (i - 28) % 7, half = (i - 28) / 7;
        dma<dst>(r2, vw2, s20 + mod7(r + j) * (128 * 896) + half * (64 * 896), ldsw);
      }
    } else if constexpr (k == K_P) {
      const u32x4 sent = {SENT, SENT, SENT, SENT};
      st_sc1(sent, rh, vl16, sp_out + i * 1024);
    } else if constexpr (k == K_SA) st_sc1(hf[i], rh, vl16, sh_own + i * 1024);
    else if constexpr (k == K_SB) st_sc1(hf[2 + i], rh, vl16, sh_own + 4096 + i * 1024);
    else if constexpr (k == K_FL) {
      const unsigned vf = CTRL_OFF + 256 + flag_slot(i) * 8;
      asm volatile("global_load_dwordx2 %0, %1, %2 sc1" : "=&v"(flv) : "v"(vf), "s"(ws) : "memory");
    } else if constexpr (k == K_LA) ld_sc1(hf[4 + i], rh, vl16, hoff(IC<4 + i>{}));
    else if constexpr (k == K_LB) ld_sc1(hf[16 + i], rh, vl16, hoff(IC<16 + i>{}));
    else if constexpr (k == K_PS) st_sc1(pout[i], rh, vl16, sp_out + i * 1024);
    else if constexpr (k == K_PL) ld_sc1(pin_[i], rh, vl16, sp_in + i * 1024);
  };
  auto emit = [&](auto P) {
    constexpr int pos = decltype(P)::value;
    static_for<NV>([&](auto I) { constexpr int i = decltype(I)::value; if constexpr (VSEQ[i].pos == pos) do_op(IC<VSEQ[i].op>{}); });
  };

  emit(IC<PRO>{});
  SB();
  const CAS u32x8* b1p = (const CAS u32x8*)(b1 + (128 * fs + 32 * ft) * 2);
  const u32x8 b1a0 = b1p[0], b1a1 = b1p[1], b1b0 = b1p[4], b1b1 = b1p[5];
  const CAS u32x4* b2p = (const CAS u32x4*)(b2 + (64 * r + 32 * kh + 16 * ft) * 2);
  const u32x4 b2s0 = b2p[0], b2s1 = b2p[1];
  asm volatile("" ::"s"(out));
  GAS u64* ctrl = (GAS u64*)(ws + CTRL_OFF);
  const u64 tag = mix64(*(const CAS u64*)(ws + CTRL_OFF));

  const int blk = (l >> 4) & 1, q = (l & 15) >> 2, p4 = l & 3;
  unsigned xrd[4], w1rd[3], w2rd[2][3];
  _Pragma("unroll") for (int s = 0; s < 4; ++s)
      xrd[s] = (mt >> 1) * UNIT + (32 * (mt & 1) + (l & 31)) * 128 + (((2 * s + h) ^ ((l >> 1) & 7)) << 4);
  w1rd[0] = (8 * h + q) * 128 + ((64 * ft + 32 * (p4 & 1) + 16 * blk + 8 * (p4 >> 1)) ^ ((q >> 1) << 6));
  w1rd[1] = w1rd[0] + 65536; w1rd[2] = w1rd[0] + 131072;
  pin(w1rd[0]); pin(w1rd[1]); pin(w1rd[2]);

  bf16x8 xf[28];
  bf16x8 wf[2][4];
  f32x16 acc1[2], acc2[2];
  _Pragma("unroll") for (int i = 0; i < 8; ++i) {
    const unsigned da = h ? b1a1[i] : b1a0[i], db = h ? b1b1[i] : b1b0[i];
    acc1[0][2 * i] = bflo(da); acc1[0][2 * i + 1] = bfhi(da); acc1[1][2 * i] = bflo(db); acc1[1][2 * i + 1] = bfhi(db);
  }

  auto trd2 = [&](unsigned base, auto Off) {
    constexpr int off = decltype(Off)::value;
    s16x4 lo = __builtin_amdgcn_ds_read_tr16_b64_v4i16((LAS s16x4*)(lds + base + off)), hi = __builtin_amdgcn_ds_read_tr16_b64_v4i16((LAS s16x4*)(lds + base + off + 512));
    return __builtin_shufflevector(lo, hi, 0, 1, 2, 3, 4, 5, 6, 7);
  };
  auto reads = [&](auto S, auto Hf) {
    constexpr int s = decltype(S)::value, hh = decltype(Hf)::value;
    constexpr int wo = slot_of(last_unit_of_reads(s)) * UNIT;
    if constexpr (s < 7 && hh == 0 && (s == 3 || s == 6)) { _Pragma("unroll") for (int k = 0; k < 4; ++k) { xrd[k] += 65536; pin(xrd[k]); } }
    static_for<2>([&](auto K) {
      constexpr int k = 2 * hh + decltype(K)::value;
      if constexpr (s < 7) xf[4 * s + k] = *(const bf16x8*)(lds + xrd[k] + ((3 * s * UNIT) & 0xffff));
      wf[s & 1][k] = trd2(w1rd[wo >> 16], IC<(wo & 0xffff) + k * 2048>{});
    });
  };

  wait_vm_bar<vm_for(D_(2), TOP(0))>();
  STAMP(1);
  reads(IC<0>{}, IC<0>{}); reads(IC<0>{}, IC<1>{});

  bf16x8 w2f[3];
  auto rd2 = [&](auto I) {
    constexpr int i = decltype(I)::value;
    if constexpr (i < 56) {
      constexpr int f = item(i).frag, wo = slot_of(28 + 7 * frag_half(f) + frag_j(f)) * UNIT;
      w2f[i % 3] = trd2(w2rd[item(i).tile][wo >> 16], IC<(wo & 0xffff) + frag_s(f) * 1024>{});
    }
  };
  auto flag_check = [&](auto I, auto Pos) {
    constexpr int i = decltype(I)::value;
    wait_vm<vm_for(OP(K_FL, i), decltype(Pos)::value)>();
    pin(flv);
    if (__builtin_expect(__builtin_amdgcn_ballot_w64(flv != tag) != 0, 0)) {
      for (int spin = 0; spin < SPIN_CAP; ++spin) {
        const u64 f = __hip_atomic_load(ctrl + 32 + flag_slot(i), __ATOMIC_RELAXED, __HIP_MEMORY_SCOPE_AGENT);
        RETRY_NOTE(i ? 6 : 0);
        if (__builtin_amdgcn_ballot_w64(f != tag) == 0) break;
        __builtin_amdgcn_s_sleep(1);
      }
    }
  };
  static_for<14>([&](auto S) {
    constexpr int s = decltype(S)::value, ps = s / 7, c = s % 7;
    if constexpr (BAR[s]) wait_vm_bar<cnt_before(TOP(s)) - 1 - top_need(s)>(); else SB();
    STAMP(2 + s);
    if constexpr (s == FLAGPUB) {
      if (tid == 0) __hip_atomic_store(ctrl + 32 + g * 16 + fs, tag, __ATOMIC_RELAXED, __HIP_MEMORY_SCOPE_AGENT);
      const unsigned t = (32 * ft + 16 * h + q) * 128 + ((16 * (p4 & 1) + 32 * (blk ^ ft) + 8 * (p4 >> 1)) ^ ((q >> 1) << 6));
      _Pragma("unroll") for (int t3 = 0; t3 < 3; ++t3) { w2rd[0][t3] = (t ^ (64 * (1 - kh))) + t3 * 65536; w2rd[1][t3] = (t ^ (64 * kh)) + t3 * 65536; pin(w2rd[0][t3]); pin(w2rd[1][t3]); }
    }
    emit(IC<TOP(s)>{});
    static_for<4>([&](auto K) {
      constexpr int k = decltype(K)::value;
      if constexpr (s < 13 && k < 2) { reads(IC<s + 1>{}, K); SB(); }
      if constexpr (s == 13 && k >= 2) { rd2(IC<k - 2>{}); SB(); }
      acc1[ps] = MFMA(wf[s & 1][k], xf[4 * c + k], acc1[ps]);
      SB();
      static_for<8>([&](auto J) {
        constexpr int j = decltype(J)::value;
        if constexpr (s >= 7 && ga_at(j) == 4 * (s - 7) + k) { hf[j >> 2][j & 3] = gelu2(acc1[0][2 * j], acc1[0][2 * j + 1]); SB(); }
      });
      if constexpr (ST(s, k) == pos_of(OP(K_SA, 0))) flag_check(IC<0>{}, IC<ST(s, k)>{});
      emit(IC<ST(s, k)>{});
      SB();
    });
  });
  STAMP(16);
  _Pragma("unroll") for (int i = 0; i < 16; ++i) { acc2[0][i] = 0.f; acc2[1][i] = 0.f; }
  auto check6 = [&](auto F0, auto Pos) {
    constexpr int f0 = decltype(F0)::value, op = f0 < 16 ? OP(K_LA, f0 - 4 + 5) : OP(K_LB, f0 - 16 + 5);
    wait_vm<vm_for(op, decltype(Pos)::value)>();
    unsigned m = 0;
    _Pragma("unroll") for (int i = 0; i < 6; ++i) { pin(hf[f0 + i]); m = max4(m, hf[f0 + i]); }
    if (__builtin_expect(any_sent(m), 0)) {
      for (int spin = 0; spin < SPIN_CAP; ++spin) {
        static_for<6>([&](auto I) { ld_sc1(hf[f0 + decltype(I)::value], rh, vl16, hoff(IC<f0 + decltype(I)::value>{})); });
        wait_vm<0>();
        m = 0;
        _Pragma("unroll") for (int i = 0; i < 6; ++i) { pin(hf[f0 + i]); m = max4(m, hf[f0 + i]); }
        RETRY_NOTE(1 + (f0 - 4) / 6);
        if (!any_sent(m)) break;
      }
    }
  };
  char* const redP = lds + 2 * UNIT + l * 16, * const redO = lds + 4 * UNIT + l * 16;
  auto red_write = [&](char* red, const f32x16& a) {
    _Pragma("unroll") for (int i = 0; i < 2; ++i) {
      f32x4 v; _Pragma("unroll") for (int e = 0; e < 4; ++e) v[e] = a[8 + 4 * i + e];
      *(f32x4*)(red + w * 2048 + i * 1024) = v;
    }
  };
  static_for<56>([&](auto I) {
    constexpr int i = decltype(I)::value;
    if constexpr (i == 4) { SB(); check6(IC<4>{}, IC<MF(i)>{}); STAMP(17); }
    if constexpr (i == 16) { SB(); check6(IC<10>{}, IC<MF(i)>{}); STAMP(18); }
    if constexpr (i == BARB - 2) { wait_vm_bar<vm_for(D_(41), MF(i))>(); STAMP(19); }
    if constexpr (i == 30) { SB(); STAMP(20); check6(IC<16>{}, IC<MF(i)>{}); STAMP(21); }
    if constexpr (i == 36 + FILL) { SB(); STAMP(22); check6(IC<22>{}, IC<MF(i)>{}); STAMP(23); }
    if constexpr (i == SEND_W) { red_write(redP, acc2[0]); STAMP(24); }
    if constexpr (i == SEND_R) {
      wait_vm_bar<63>();
      _Pragma("unroll") for (int j = 0; j < 2; ++j) {
        const f32x4 o = *(const f32x4*)(redP + (w ^ 4) * 2048 + j * 1024);
        _Pragma("unroll") for (int e = 0; e < 4; ++e) pout[j][e] = nosent(__float_as_uint(acc2[0][4 * j + e] + o[e]));
      }
      STAMP(25);
    }
    if constexpr (MF(i) == pos_of(OP(K_PL, 0))) flag_check(IC<1>{}, IC<MF(i)>{});
    SB();
    emit(IC<MF(i)>{});
    rd2(IC<i + 2>{});
    SB();
    acc2[item(i).tile] = MFMA(w2f[i % 3], __builtin_bit_cast(bf16x8, hf[item(i).frag]), acc2[item(i).tile]);
    SB();
    static_for<8>([&](auto J) {
      constexpr int j = decltype(J)::value;
      if constexpr (gb_at(j) == i) { hf[2 + (j >> 2)][j & 3] = gelu2(acc1[1][2 * j], acc1[1][2 * j + 1]); SB(); }
    });
  });
  STAMP(26);
  red_write(redO, acc2[1]);
  wait_vm_bar<63>();
  STAMP(27);
  {
    const f32x4 o0 = *(const f32x4*)(redO + (w ^ 4) * 2048), o1 = *(const f32x4*)(redO + (w ^ 4) * 2048 + 1024);
    float s[8];
    _Pragma("unroll") for (int e = 0; e < 4; ++e) {
      const unsigned d = h ? b2s1[e] : b2s0[e];
      s[2 * e] = ((e < 2 ? o0[2 * e] : o1[2 * e - 4]) + acc2[1][2 * e]) + bflo(d);
      s[2 * e + 1] = ((e < 2 ? o0[2 * e + 1] : o1[2 * e - 3]) + acc2[1][2 * e + 1]) + bfhi(d);
    }
    wait_vm<vm_for(OP(K_PL, 1), FIN)>();
    pin(pin_[0]); pin(pin_[1]);
    if (__builtin_expect(any_sent(max4(max4(0, pin_[0]), pin_[1])), 0)) {
      for (int spin = 0; spin < SPIN_CAP; ++spin) {
        ld_sc1(pin_[0], rh, vl16, sp_in); ld_sc1(pin_[1], rh, vl16, sp_in + 1024);
        wait_vm<0>();
        pin(pin_[0]); pin(pin_[1]);
        RETRY_NOTE(5);
        if (!any_sent(max4(max4(0, pin_[0]), pin_[1]))) break;
      }
    }
    STAMP(28);
    u32x4 o;
    _Pragma("unroll") for (int e = 0; e < 4; ++e)
        o[e] = pack2(s[2 * e] + __uint_as_float(pin_[e >> 1][(2 * e) & 3]), s[2 * e + 1] + __uint_as_float(pin_[e >> 1][(2 * e + 1) & 3]));
    const unsigned row = 128 * g + 32 * mt + (l & 31);
    *(u32x4*)(out + row * 896 + (64 * r + 32 * kh + 16 * ft + 8 * h) * 2) = o;
  }
}
}

extern "C" void kernel_launch(void* const* d_in, const int* in_sizes, int n_in, void* d_out, int out_size, void* d_ws, size_t ws_size,
                              hipStream_t stream) {
  ffn::ffn_kernel<<<ffn::NWORK + 32, 512, 0, stream>>>((const char*)d_in[0], (const char*)d_in[1], (const char*)d_in[2], (const char*)d_in[3],
                                                      (const char*)d_in[4], (char*)d_out, (char*)d_ws);
}
```

```cpp
#include <hip/hip_runtime.h>
#include <stdint.h>
#include <utility>

typedef short bf16x8 __attribute__((ext_vector_type(8)));
typedef short s16x4 __attribute__((ext_vector_type(4)));
typedef float f32x16 __attribute__((ext_vector_type(16)));
typedef float f32x4 __attribute__((ext_vector_type(4)));
typedef float f32x2 __attribute__((ext_vector_type(2)));
typedef __bf16 bf16x2_t __attribute__((ext_vector_type(2)));
typedef unsigned u32x4 __attribute__((ext_vector_type(4)));
typedef unsigned u32x8 __attribute__((ext_vector_type(8)));
typedef unsigned long long u64;
#define LAS __attribute__((address_space(3)))
#define GAS __attribute__((address_space(1)))
#define CAS __attribute__((address_space(4)))
#define DI __device__ __forceinline__

namespace ffn {
constexpr int M = 2048, D = 448, F = 1792;
constexpr int NWORK = 224;
constexpr int NSLOT = 20, NUNIT = 42, UNIT = 8192;
constexpr unsigned CTRL_OFF = 12u << 20;
constexpr unsigned FLAG_OFF = CTRL_OFF + 4096;
constexpr unsigned WS_BYTES = 13u << 20;
constexpr unsigned SENT = 0xFFFFFFFFu;
#define LAY 1835008, 8192, 131072, 1024
constexpr unsigned LAYV[4] = {LAY}, SO = LAYV[0], SG = LAYV[1], SF = LAYV[2], SW = LAYV[3];
static_assert(5 * SO + 15 * SG + 13 * SF + 7 * SW + 1024 <= CTRL_OFF, "layout");
constexpr int CODE_LINES = 80;
constexpr int SPIN_CAP = 1 << 16;

#define SCHED 0
#define OST 0
#define LATE 0
#define NPOLL 1

enum { K_D = 0, K_HS, K_P, K_FL, K_L, K_PS, K_PL };
constexpr int OP(int k, int i) { return k * 64 + i; }
constexpr int PRO = 0, FIRST = 7;
constexpr int TOP(int s) { return 8 + 8 * s; }
constexpr int ST(int s, int k) { return 8 + 8 * s + 1 + k; }
constexpr int PB(int m) { return ST(7 + m / 4, m % 4); }
constexpr int GB(int j) { return 128 + j; }
constexpr int MF(int i) { return 160 + i; }
constexpr int END = 230;
struct Ent { int op, pos; };
#define D_(u) OP(K_D, u)
#define L_(f) OP(K_L, f)
#define P_(i) OP(K_P, i)
#define L6(f, p0, p1, p2, p3, p4, p5) {L_(f), p0}, {L_(f + 1), p1}, {L_(f + 2), p2}, {L_(f + 3), p3}, {L_(f + 4), p4}, {L_(f + 5), p5}
#define L6X(f, ...) L6(f, __VA_ARGS__)
#define GA_M 13
#define GA_E 26
#define FILLS 4, 4, 4, 2
#define P_FL0 ST(12, 1)
#define P_FL1 GB(2)
#define P_L0 ST(13, 1), ST(13, 3), GB(0), GB(1), GB(1), GB(2)
#define P_L1 GB(6), GB(7), MF(0), MF(1), MF(2), MF(3)
#define I_L2 8
#define I_L3 18
#define D_PL 8, 12, 13
#define PUBS 9, 12
#define PPOS PRO + 2
constexpr int NFL = SCHED == 0 ? 2 : 1;
#define W2BAR 5
#define BARS 1, 1, 1, 1, 1, 1, 1, 1, 1, 1, 0, 0, 1, 0
constexpr int ga_at(int j) { return j <= 8 ? j * GA_M / 8 : GA_M + (j - 8) * (GA_E - GA_M) / 8; }
constexpr int FILL[4] = {FILLS};
constexpr int wait_at(int g) { int m = 4; for (int i = 0; i <= g; ++i) m += FILL[i] + (i ? 6 : 0); return m; }
constexpr int SEND_W = wait_at(3) + 6, SEND_R = SEND_W + 2;
constexpr int DPL[3] = {D_PL};
constexpr int I_PL(int n) { return SEND_R + DPL[n]; }
constexpr int PUB[2] = {PUBS};
constexpr Ent VRAW[] = {
  {D_(0), PRO}, {D_(1), PRO}, {D_(2), PRO}, {D_(3), PRO + 1}, {D_(4), PRO + 1}, {D_(5), PRO + 1},
  {D_(6), FIRST}, {D_(7), FIRST}, {D_(8), FIRST},
  {D_(9), ST(0, 0)}, {D_(10), ST(0, 1)}, {D_(11), ST(0, 2)}, {D_(12), ST(0, 3)},
  {D_(13), ST(1, 0)}, {D_(14), ST(1, 1)}, {D_(15), ST(1, 2)}, {D_(16), ST(1, 3)},
  {D_(17), ST(2, 0)}, {D_(18), ST(2, 1)}, {D_(19), ST(2, 2)}, {D_(20), ST(2, 3)},
  {D_(21), ST(3, 0)}, {D_(22), ST(3, 1)}, {P_(0), ST(3, 3)},
  {D_(23), ST(4, 0)}, {D_(24), ST(4, 2)},
  {D_(25), ST(5, 0)}, {P_(1), ST(5, 1)}, {D_(26), ST(5, 3)},
  {D_(27), ST(6, 0)}, {D_(28), ST(6, 2)},
  {D_(29), ST(7, 0)}, {P_(2), ST(7, 1)}, {D_(30), ST(7, 3)},
  {D_(31), ST(8, 0)}, {D_(32), ST(8, 2)},
  {D_(33), ST(9, 0)}, {P_(3), ST(9, 1)}, {D_(34), ST(9, 3)},
  {D_(35), ST(10, 0)}, {D_(36), ST(10, 2)},
  {D_(37), ST(11, 0)}, {D_(38), ST(11, 2)},
  {D_(39), ST(12, 0)}, {D_(40), ST(12, 2)},
  {D_(41), ST(12, 3)},
  {OP(K_FL, 0), P_FL0},
  {OP(K_HS, 0), PB(ga_at(8))}, {OP(K_HS, 1), PB(ga_at(16))},
  L6X(4, P_L0),
  {OP(K_FL, 1), P_FL1},
  {OP(K_HS, 2), GB(3)},
  {OP(K_HS, 3), GB(7)},
  L6X(10, P_L1),
  L6(16, MF(I_L2), MF(I_L2 + 1), MF(I_L2 + 2), MF(I_L2 + 3), MF(I_L2 + 4), MF(I_L2 + 5)),
  L6(22, MF(I_L3), MF(I_L3 + 1), MF(I_L3 + 2), MF(I_L3 + 3), MF(I_L3 + 4), MF(I_L3 + 5)),
  {OP(K_PS, 0), MF(SEND_R)}, {OP(K_PS, 1), MF(SEND_R)},
  {OP(K_PL, 0), MF(I_PL(0))}, {OP(K_PL, 1), MF(I_PL(0))},
};
constexpr int NV = sizeof(VRAW) / sizeof(VRAW[0]);
struct Prog { Ent e[NV]; constexpr const Ent& operator[](int i) const { return e[i]; } };
constexpr Prog sort_prog() {
  Prog p{}; int n = 0;
  for (int pos = 0; pos <= END; ++pos) for (int i = 0; i < NV; ++i) if (VRAW[i].pos == pos) p.e[n++] = VRAW[i];
  return p;
}
constexpr Prog VSEQ = sort_prog();
constexpr bool BAR[14] = {BARS};
constexpr int idx_of(int op) { for (int i = 0; i < NV; ++i) if (VSEQ[i].op == op) return i; return -1000; }
constexpr int pos_of(int op) { return VSEQ[idx_of(op)].pos; }
constexpr int cnt_before(int pos) { int n = 0; for (int i = 0; i < NV; ++i) if (VSEQ[i].pos < pos) ++n; return n; }
constexpr int vm_for(int op, int pos) { return cnt_before(pos) - 1 - idx_of(op); }
constexpr int slot_of(int u) { return u % NSLOT; }
constexpr int last_unit_of_reads(int s) { return s < 7 ? 3 * s + 2 : 21 + (s - 7); }
constexpr int use_step(int u) { return u < 21 ? u / 3 : u < 28 ? u - 21 + 7 : 99; }
constexpr int next_bar(int s) { int t = s + 1; while (t < 14 && !BAR[t]) ++t; return t; }
constexpr int top_need(int s) {
  int nb = next_bar(s), last = nb < 14 ? nb : 13, m = -1;
  for (int c = s + 1; c <= last; ++c) for (int u = (c < 7 ? 3 * c : 21 + c - 7); u <= last_unit_of_reads(c); ++u) if (idx_of(D_(u)) > m) m = idx_of(D_(u));
  for (int i = 0; i < 4; ++i) if (s >= PUB[i >= 2] && idx_of(P_(i)) > m) m = idx_of(P_(i));
  return m;
}
constexpr int last_w2() { int m = 28; for (int u = 28; u < 42; ++u) if (idx_of(D_(u)) > idx_of(D_(m))) m = u; return m; }
constexpr int flag_user(int st) { return L_(4 + 12 * st); }
constexpr bool check_prog() {
  for (int u = NSLOT; u < NUNIT; ++u) {
    int us = use_step(u - NSLOT), p = pos_of(D_(u)); bool ok = false;
    for (int t = us + 1; t < 14; ++t) if (BAR[t] && TOP(t) <= p) ok = true;
    if (!ok) return false;
  }
  for (int s = 0; s < 14; ++s) if (BAR[s] && top_need(s) >= cnt_before(TOP(s))) return false;
  if (!BAR[0] || !BAR[1] || ga_at(16) > 27 || W2BAR > 6) return false;
  for (int u = 28; u < 42; ++u) if (pos_of(D_(u)) >= GB(W2BAR)) return false;
  for (int st = 0; st < NFL; ++st) {
    if (idx_of(flag_user(st)) < idx_of(OP(K_FL, st)) || idx_of(L_(4 + 12 * st)) < idx_of(flag_user(st))) return false;
    if (!BAR[PUB[st]] || pos_of(OP(K_FL, st)) < TOP(PUB[st])) return false;
  }
  for (int i = 0; i < 4; ++i) if (idx_of(P_(i)) > idx_of(OP(K_HS, i)) || (i && idx_of(OP(K_HS, i)) < idx_of(OP(K_HS, i - 1)))) return false;
  for (int f = 5; f < 28; ++f) if (idx_of(L_(f)) < idx_of(L_(f - 1))) return false;
  for (int g = 0, o = 0; g < 4; ++g) { o += FILL[g]; if (pos_of(L_(9 + 6 * g)) >= MF(wait_at(g) - 1) || o > 4 + 6 * g) return false; }
  if (SEND_R > 55) return false;
  for (int n = 0; n < NPOLL; ++n) if (I_PL(n) > 57 || (n && I_PL(n) < I_PL(n - 1))) return false;
  return true;
}
static_assert(check_prog(), "schedule");
struct Item { int tile, frag; };
constexpr Item item(int i) {
  int p = 0, o = 0;
  for (int n = 0;; ++n) {
    bool own = p >= 28;
    for (int g = 0; g < 4; ++g) if (p == 4 + 6 * g && n < wait_at(g)) own = true;
    if (n == i) return Item{own, own ? o : p};
    if (own) ++o; else ++p;
  }
}
constexpr int frag_j(int f) { return f < 4 ? 0 : 1 + (f - 4) % 6; }
constexpr int frag_q(int f) { return f < 4 ? f : (f - 4) / 6; }
constexpr int clampv(int n) { return n > 63 ? 63 : n; }

template <int N> DI void wait_vm() { static_assert(N >= 0, "neg"); asm volatile("s_waitcnt vmcnt(%0)" ::"i"(clampv(N)) : "memory"); }
#define SB() __builtin_amdgcn_sched_barrier(0)
template <int N> DI void vm_bar() {
  static_assert(N >= 0, "neg");
  SB(); asm volatile("s_waitcnt vmcnt(%0)\n\ts_barrier" ::"i"(clampv(N)) : "memory"); SB();
}
DI void lds_bar() {
  SB(); __builtin_amdgcn_s_waitcnt(0xC07F); asm volatile("s_waitcnt lgkmcnt(0)\n\ts_barrier" ::: "memory"); SB();
}
template <class T> DI void pin(T& x) { asm volatile("" : "+v"(x)); }
template <class T> DI void pins(T& x) { asm volatile("" : "+s"(x)); }

template <int OFF> DI void dma(u32x4 rsrc, unsigned voff, unsigned soff, unsigned ldsw) {
  asm volatile("s_add_u32 m0, %3, %4\n\ts_nop 0\n\tbuffer_load_dwordx4 %0, %1, %2 offen lds" ::"v"(voff), "s"(rsrc), "s"(soff), "s"(ldsw), "i"(OFF) : "memory", "scc");
}
DI u32x4 make_rsrc(const void* p, unsigned bytes) {
  u64 a = (u64)p; u32x4 r; r.x = (unsigned)a; r.y = (unsigned)(a >> 32) & 0xffffu; r.z = bytes; r.w = 0x00020000u; return r;
}
DI void st_sc1(u32x4 v, u32x4 rsrc, unsigned voff, unsigned soff) {
  asm volatile("buffer_store_dwordx4 %0, %1, %2, %3 offen sc1\n\ts_nop 1" ::"v"(v), "v"(voff), "s"(rsrc), "s"(soff) : "memory");
}
DI void ld_sc1(u32x4& a, u32x4 rsrc, unsigned voff, unsigned soff) {
  asm volatile("buffer_load_dwordx4 %0, %1, %2, %3 offen sc1" : "=&v"(a) : "v"(voff), "s"(rsrc), "s"(soff) : "memory");
}
#define LDP(n, reg) if constexpr (I == n) asm volatile("buffer_load_dwordx4 %0, %1, %2, %3 offen sc1" : "={" reg "}"(a) : "v"(voff), "s"(rsrc), "s"(soff) : "memory")
template <int I> DI void ld_poll(u32x4& a, u32x4 rsrc, unsigned voff, unsigned soff) {
  LDP(0, "v[208:211]"); LDP(1, "v[212:215]"); LDP(2, "v[216:219]"); LDP(3, "v[220:223]"); LDP(4, "v[224:227]"); LDP(5, "v[228:231]");
}
DI void flag_st(u64 tag, u32x4 rsrc, unsigned soff) {
  asm volatile("s_mov_b64 exec, 1\n\tbuffer_store_dwordx2 %0, off, %1, %2 sc1\n\ts_mov_b64 exec, -1\n\ts_nop 0" ::"v"(tag), "s"(rsrc), "s"(soff) : "memory");
}
template <int MASK> DI void flag_ld(u64& f, u32x4 rsrc, unsigned voff, unsigned soff) {
  asm volatile("s_mov_b64 exec, %4\n\tbuffer_load_dwordx2 %0, %1, %2, %3 offen sc1\n\ts_mov_b64 exec, -1" : "+v"(f) : "v"(voff), "s"(rsrc), "s"(soff), "i"(MASK) : "memory");
}
DI void touch(unsigned& t, const char* p) { asm volatile("global_load_dword %0, %1, off" : "=&v"(t) : "v"(p) : "memory"); }
DI u64 mix64(u64 z) { z += 0x9E3779B97F4A7C15ull; z = (z ^ (z >> 30)) * 0xBF58476D1CE4E5B9ull; z = (z ^ (z >> 27)) * 0x94D049BB133111EBull; return z ^ (z >> 31); }
constexpr float GC0 = -2.0f * 0.7978845608028654f * 1.4426950408889634f, GC1 = GC0 * 0.044715f;
DI unsigned pack2(float a, float b) { f32x2 v = {a, b}; return __builtin_bit_cast(unsigned, __builtin_convertvector(v, bf16x2_t)); }
DI unsigned nosent(unsigned p) { return p < SENT ? p : SENT - 1; }
DI unsigned umax3(unsigned a, unsigned b, unsigned c) { unsigned m = a > b ? a : b; return m > c ? m : c; }
DI unsigned max8(u32x4 a, u32x4 b) { return umax3(umax3(a.x, a.y, a.z), umax3(a.w, b.x, b.y), b.z > b.w ? b.z : b.w); }
DI unsigned max4(u32x4 a) { return umax3(umax3(a.x, a.y, a.z), a.w, 0u); }
DI bool any_sent(unsigned m) { return __builtin_amdgcn_ballot_w64(m == SENT) != 0; }
DI unsigned gelu2pk(float a, float b) {
  const f32x2 v = {a, b}, c0 = {GC0, GC0}, c1 = {GC1, GC1}, one = {1.f, 1.f};
  const f32x2 z = v * __builtin_elementwise_fma(v * v, c1, c0);
  const f32x2 e = {__builtin_amdgcn_exp2f(z.x), __builtin_amdgcn_exp2f(z.y)}, d = e + one;
  const f32x2 rc = {__builtin_amdgcn_rcpf(d.x), __builtin_amdgcn_rcpf(d.y)};
  unsigned p = nosent(__builtin_bit_cast(unsigned, __builtin_convertvector(v * rc, bf16x2_t)));
  asm volatile("" : "+v"(p));
  return p;
}
DI float bflo(unsigned d) { return __uint_as_float(d << 16); }
DI float bfhi(unsigned d) { return __uint_as_float(d & 0xffff0000u); }
template <class Fn, int... I> DI void static_for_impl(Fn&& f, std::integer_sequence<int, I...>) { (f(std::integral_constant<int, I>{}), ...); }
template <int N, class Fn> DI void static_for(Fn&& f) { static_for_impl(f, std::make_integer_sequence<int, N>{}); }
template <int V> using IC = std::integral_constant<int, V>;
#define MFMA(a, b, c) __builtin_amdgcn_mfma_f32_32x32x16_bf16((a), (b), (c), 0, 0, 0)

#define STAMP(i) do {} while (0)
#define RETRY_NOTE(k) do {} while (0)
#define HST(i) do {} while (0)

__global__ __launch_bounds__(512, 2) void ffn_kernel(const char* __restrict__ x, const char* __restrict__ w1, const char* __restrict__ b1,
                                                     const char* __restrict__ w2, const char* __restrict__ b2, char* __restrict__ out, char* ws) {
  __shared__ __attribute__((aligned(16))) char lds[NSLOT * UNIT];
  u64 pc; asm volatile("s_getpc_b64 %0" : "=s"(pc));
  const int b = blockIdx.x;
  const int tid = threadIdx.x, l = tid & 63;
  const int w = __builtin_amdgcn_readfirstlane(tid >> 6);
  if (b >= NWORK) {
    const int hj = b - NWORK;
    HST(0);
    GAS u64* const ctrl = (GAS u64*)(ws + CTRL_OFF);
    {
      unsigned t0, t1;
      auto a2 = [&](int n, int hf) { return w2 + (n / 448) * 114688 + hf * 57344 + (n % 448) * 128; };
      const int n = 196 * hj + (tid < 196 ? tid : tid < 392 ? tid - 196 : 0);
      touch(t0, tid < 196 ? w1 + n * 256 + 128 : tid < 392 ? a2(n, 0) : (const char*)(pc & ~255ull) + (tid < 392 + CODE_LINES ? tid - 392 : 0) * 128);
      touch(t1, a2(196 * hj + (tid < 196 ? tid : 0), 1));
      asm volatile("s_waitcnt vmcnt(0)" ::"v"(t0), "v"(t1) : "memory");
      HST(1);
    }
    if (hj == 0 && w == 7) {
      const u64 e = __hip_atomic_load(ctrl, __ATOMIC_RELAXED, __HIP_MEMORY_SCOPE_AGENT), tag = mix64(e);
      for (int spin = 0; spin < SPIN_CAP; ++spin) {
        bool ok = true;
        _Pragma("unroll") for (int i = 0; i < 4; ++i) {
          const int j = l + 64 * i;
          if ((j & 7) < 7) ok &= __hip_atomic_load((GAS u64*)(ws + FLAG_OFF + (j >> 3) * 256 + (j & 7) * 8), __ATOMIC_RELAXED, __HIP_MEMORY_SCOPE_AGENT) == tag;
        }
        if (__builtin_amdgcn_ballot_w64(!ok) == 0) break;
        __builtin_amdgcn_s_sleep(16);
      }
      if (l == 0) __hip_atomic_store(ctrl, e + 1, __ATOMIC_RELAXED, __HIP_MEMORY_SCOPE_AGENT);
    }
    return;
  }
  auto mod7 = [](int v) { return v >= 7 ? v - 7 : v; };
  const int xcd = b & 7, idx = b >> 3, qd = (idx * 37) >> 8, r = idx - 7 * qd;
  const int kh = xcd & 1, g = 4 * (xcd >> 1) + qd, fs = 7 * kh + r, rot = mod7(r + 2 * qd);
  const u32x4 rx = make_rsrc(x, M * D * 2), r1 = make_rsrc(w1, D * F * 2), r2 = make_rsrc(w2, D * F * 2), rh = make_rsrc(ws, WS_BYTES);
  const unsigned ldsw = (unsigned)(size_t)(LAS char*)lds + w * 1024;

  const unsigned prow = 8 * w + (l >> 3);
  const unsigned vx = prow * 896 + (((l & 7) ^ (((w & 1) << 2) | (l >> 4))) << 4);
  const unsigned wsw = ((l & 7) ^ (((l >> 4) & 1) << 2)) << 4;
  const unsigned vw1 = prow * 3584 + wsw;
  const unsigned sx0 = g * 128 * 896, s10 = fs * 256;
  unsigned vw2, s20, vl16, vfl, sh_row, sh_own, sp_out, sp_in, sf_g;

  u32x4 hf[28];
  u32x4 pin_[2 * NPOLL], pout[2];
  u64 tag, tagv, flv[2];
  auto hoff = [&](auto Fr) { constexpr int f = decltype(Fr)::value; return sh_row + mod7(r + frag_j(f)) * SF + frag_q(f) * SO; };
  auto flag_load = [&](auto St) {
    constexpr int st = decltype(St)::value;
    flv[st] = tagv;
    flag_ld<0x7f>(flv[st], rh, vfl, sf_g + st * 128);
  };
  auto flag_check = [&](auto St, auto N) {
    constexpr int st = decltype(St)::value;
    wait_vm<decltype(N)::value>();
    pin(flv[st]);
    if (__builtin_expect(__builtin_amdgcn_ballot_w64(flv[st] != tag) != 0, 0)) {
      for (int spin = 0; spin < SPIN_CAP; ++spin) {
        flag_load(St);
        wait_vm<0>();
        pin(flv[st]);
        RETRY_NOTE(st);
        if (__builtin_amdgcn_ballot_w64(flv[st] != tag) == 0) break;
      }
    }
  };
  auto do_op = [&](auto Ix) {
    constexpr int ix = decltype(Ix)::value, op = VSEQ[ix].op, k = op / 64, i = op % 64;
    if constexpr (op == flag_user(0) || (op == flag_user(1) && NFL == 2)) flag_check(IC<op == flag_user(1)>{}, IC<ix - 1 - idx_of(OP(K_FL, op == flag_user(1)))>{});
    if constexpr (k == K_D) {
      constexpr int dst = slot_of(i) * UNIT;
      if constexpr (i < 21) {
        constexpr int c = i / 3, kk = i % 3;
        if constexpr (kk < 2) dma<dst>(rx, vx, sx0 + kk * 64 * 896 + mod7(c + rot) * 128, ldsw);
        else dma<dst>(r1, vw1, s10 + mod7(c + rot) * (64 * 3584), ldsw);
      } else if constexpr (i < 28) {
        dma<dst>(r1, vw1, s10 + 128 + mod7(i - 21 + rot) * (64 * 3584), ldsw);
      } else {
        constexpr int j = (i - 28) % 7, half = (i - 28) / 7;
        dma<dst>(r2, vw2, s20 + mod7(r + j) * (128 * 896) + half * (64 * 896), ldsw);
      }
    } else if constexpr (k == K_HS) st_sc1(hf[i], rh, vl16, sh_own + i * SO);
    else if constexpr (k == K_P) {
      const u32x4 sent = {SENT, SENT, SENT, SENT};
      st_sc1(sent, rh, vl16, sh_own + i * SO);
    } else if constexpr (k == K_FL) flag_load(IC<i>{});
    else if constexpr (k == K_L) ld_sc1(hf[i], rh, vl16, hoff(IC<i>{}));
    else if constexpr (k == K_PS) st_sc1(pout[i], rh, vl16, sp_out + i * SO);
    else if constexpr (k == K_PL) ld_poll<i>(pin_[i], rh, vl16, sp_in + (i & 1) * SO);
  };
  auto emit = [&](auto P) {
    constexpr int pos = decltype(P)::value;
    static_for<NV>([&](auto I) { if constexpr (VSEQ[decltype(I)::value].pos == pos) do_op(I); });
  };

  emit(IC<PRO>{});
  SB();
  asm volatile("" ::"s"(b1), "s"(w2), "s"(b2), "s"(out), "s"(ws));
  SB();
  const int mt = w & 3, ft = w >> 2;
  const CAS u32x8* b1p = (const CAS u32x8*)(b1 + (128 * fs + 32 * ft) * 2);
  const u32x8 b1a0 = b1p[0], b1a1 = b1p[1], b1b0 = b1p[4], b1b1 = b1p[5];
  const CAS u32x4* b2p = (const CAS u32x4*)(b2 + (64 * r + 32 * kh + 16 * ft) * 2);
  const u32x4 b2s0 = b2p[0], b2s1 = b2p[1];
  u64 epoch = *(const CAS u64*)(ws + CTRL_OFF);
  SB();
  asm volatile("s_barrier" ::: "memory");
  emit(IC<PRO + 1>{});
  SB();
  STAMP(33);
  int l2 = l; pin(l2);
  const int h = l2 >> 5;
  vw2 = (8 * w + (l2 >> 3)) * 896 + (((l2 & 7) ^ (((l2 >> 4) & 1) << 2)) << 4);
  vl16 = l2 * 16; vfl = l2 * 8;
  s20 = kh * 896 * 896 + r * 128;
  sh_row = g * SG + 7 * kh * SF + w * SW; sh_own = sh_row + r * SF;
  sp_out = sh_own + 4 * SO; sp_in = sp_out + 7 * SF - kh * (14 * SF);
  sf_g = FLAG_OFF + (2 * g + kh) * 256;
  unsigned vout = (128 * g + 32 * mt + (l2 & 31)) * 896 + (64 * r + 32 * kh + 16 * ft + 8 * h) * 2;
  pin(vl16);
  SB();
  emit(IC<PRO + 2>{});
  SB();
  pin(vout); pin(vw2); pin(vfl);

  const int blk = (l2 >> 4) & 1, q = (l2 & 15) >> 2, p4 = l2 & 3;
  unsigned xrd[4], w1rd[3], w2rd[2][3];
  _Pragma("unroll") for (int s = 0; s < 4; ++s)
      xrd[s] = (mt >> 1) * UNIT + (32 * (mt & 1) + (l2 & 31)) * 128 + (((2 * s + h) ^ ((l2 >> 1) & 7)) << 4);
  w1rd[0] = (8 * h + q) * 128 + ((64 * ft + 32 * (p4 & 1) + 16 * blk + 8 * (p4 >> 1)) ^ ((q >> 1) << 6));
  w1rd[1] = w1rd[0] + 65536; w1rd[2] = w1rd[0] + 131072;
  pin(w1rd[0]); pin(w1rd[1]); pin(w1rd[2]);

  bf16x8 xf[28];
  bf16x8 wf[2][4];
  bf16x8 w2f[3];
  f32x16 acc1[2], acc2[2];
  float gprev, ge;
  _Pragma("unroll") for (int i = 0; i < 8; ++i) { const unsigned da = h ? b1a1[i] : b1a0[i]; acc1[0][2 * i] = bflo(da); acc1[0][2 * i + 1] = bfhi(da); }
  pin(acc1[0]);

  auto trd2 = [&](unsigned base, auto Off) {
    constexpr int off = decltype(Off)::value;
    s16x4 lo = __builtin_amdgcn_ds_read_tr16_b64_v4i16((LAS s16x4*)(lds + base + off)), hi = __builtin_amdgcn_ds_read_tr16_b64_v4i16((LAS s16x4*)(lds + base + off + 512));
    return __builtin_shufflevector(lo, hi, 0, 1, 2, 3, 4, 5, 6, 7);
  };
  auto reads = [&](auto S, auto K) {
    constexpr int s = decltype(S)::value, k = decltype(K)::value;
    constexpr int wo = slot_of(last_unit_of_reads(s)) * UNIT;
    if constexpr (s == 3 || s == 6) { xrd[k] += 65536; pin(xrd[k]); }
    if constexpr (s < 7) xf[4 * s + k] = *(const bf16x8*)(lds + xrd[k] + ((3 * s * UNIT) & 0xffff));
    wf[s & 1][k] = trd2(w1rd[wo >> 16], IC<(wo & 0xffff) + k * 2048>{});
  };
  auto rd2 = [&](auto I) {
    constexpr int i = decltype(I)::value;
    if constexpr (i < 56) {
      constexpr int f = item(i).frag, wo = slot_of(28 + 7 * (frag_q(f) >> 1) + frag_j(f)) * UNIT;
      w2f[i % 3] = trd2(w2rd[item(i).tile][wo >> 16], IC<(wo & 0xffff) + (frag_q(f) & 1) * 1024>{});
    }
  };

  __builtin_amdgcn_s_waitcnt(0xC07F);
  vm_bar<vm_for(D_(2), FIRST)>();
  STAMP(1);
  emit(IC<FIRST>{});
  static_for<4>([&](auto K) { reads(IC<0>{}, K); });
  SB();
  _Pragma("unroll") for (int i = 0; i < 8; ++i) { const unsigned db = h ? b1b1[i] : b1b0[i]; acc1[1][2 * i] = bflo(db); acc1[1][2 * i + 1] = bfhi(db); }
  pin(acc1[1]);
  pins(epoch); tag = mix64(epoch); tagv = tag; pin(tagv);
  auto late_setup = [&]() {
    _Pragma("unroll") for (int e = 0; e < 4; ++e) { const unsigned d = h ? b2s1[e] : b2s0[e]; acc2[1][2 * e] = bflo(d); acc2[1][2 * e + 1] = bfhi(d); }
    _Pragma("unroll") for (int e = 8; e < 16; ++e) acc2[1][e] = 0.f;
    pin(acc2[1]);
    const unsigned t = (32 * ft + 16 * h + q) * 128 + ((16 * (p4 & 1) + 32 * (blk ^ ft) + 8 * (p4 >> 1)) ^ ((q >> 1) << 6));
    _Pragma("unroll") for (int t3 = 0; t3 < 3; ++t3) { w2rd[0][t3] = (t ^ (64 * (1 - kh))) + t3 * 65536; w2rd[1][t3] = (t ^ (64 * kh)) + t3 * 65536; pin(w2rd[0][t3]); pin(w2rd[1][t3]); }
  };
  if constexpr (LATE < 0) late_setup();

  static_for<14>([&](auto S) {
    constexpr int s = decltype(S)::value, ps = s / 7, c = s % 7;
    if constexpr (BAR[s]) vm_bar<cnt_before(TOP(s)) - 1 - top_need(s)>(); else SB();
    STAMP(2 + s);
    if constexpr (s == PUB[0] || s == PUB[1]) { if (w == 0) flag_st(tagv, rh, sf_g + (s != PUB[0]) * 128 + r * 8); }
    emit(IC<TOP(s)>{});
    static_for<4>([&](auto K) {
      constexpr int k = decltype(K)::value;
      if constexpr (s < 13) { reads(IC<s + 1>{}, K); SB(); }
      acc1[ps] = MFMA(wf[s & 1][k], xf[4 * c + k], acc1[ps]);
      SB();
      static_for<17>([&](auto J) {
        constexpr int j = decltype(J)::value;
        if constexpr (s >= 7 && ga_at(j) == 4 * (s - 7) + k) {
          float en = 0.f;
          if constexpr (j < 16) { const float v = acc1[0][j]; en = __builtin_amdgcn_exp2f(v * __builtin_fmaf(v * v, GC1, GC0)); }
          if constexpr (j > 0) {
            float gv = acc1[0][j - 1] * __builtin_amdgcn_rcpf(1.0f + ge);
            if constexpr (j & 1) { gprev = gv; pin(gprev); }
            else { unsigned p = nosent(pack2(gprev, gv)); pin(p); hf[(j - 1) >> 3][((j - 1) >> 1) & 3] = p; }
          }
          ge = en; pin(ge);
        }
      });
      SB();
      emit(IC<ST(s, k)>{});
      SB();
      if constexpr (s == LATE && k == 3) { late_setup(); SB(); }
    });
  });
  STAMP(16);
  static_for<8>([&](auto J) {
    constexpr int j = decltype(J)::value;
    if constexpr (j & 1) {
      hf[2 + (j >> 2)][(j & 3) - 1] = gelu2pk(acc1[1][2 * j - 2], acc1[1][2 * j - 1]);
      hf[2 + (j >> 2)][j & 3] = gelu2pk(acc1[1][2 * j], acc1[1][2 * j + 1]);
      SB();
    }
    if constexpr (j == W2BAR) vm_bar<vm_for(D_(last_w2()), GB(j))>();
    if constexpr (j >= 6) rd2(IC<j - 6>{});
    emit(IC<GB(j)>{});
    SB();
  });
  STAMP(17);
  _Pragma("unroll") for (int i = 0; i < 16; ++i) acc2[0][i] = 0.f;
  unsigned chk;
  auto retry6 = [&](auto F0) {
    constexpr int f0 = decltype(F0)::value;
    if (__builtin_expect(any_sent(chk), 0)) {
      for (int spin = 0; spin < SPIN_CAP; ++spin) {
        static_for<6>([&](auto I) { ld_sc1(hf[f0 + decltype(I)::value], rh, vl16, hoff(IC<f0 + decltype(I)::value>{})); });
        wait_vm<0>();
        _Pragma("unroll") for (int i = 0; i < 6; ++i) pin(hf[f0 + i]);
        RETRY_NOTE(2 + (f0 - 4) / 6);
        if (!any_sent(umax3(max8(hf[f0], hf[f0 + 1]), max8(hf[f0 + 2], hf[f0 + 3]), max8(hf[f0 + 4], hf[f0 + 5])))) break;
      }
    }
  };
  char* const redP = lds + 2 * UNIT + vl16, * const redO = lds + 4 * UNIT + vl16;
  auto red_write = [&](char* red, const f32x16& a) {
    _Pragma("unroll") for (int i = 0; i < 2; ++i) {
      f32x4 v; _Pragma("unroll") for (int e = 0; e < 4; ++e) v[e] = a[8 + 4 * i + e];
      *(f32x4*)(red + w * 2048 + i * 1024) = v;
    }
  };
  static_for<56>([&](auto I) {
    constexpr int i = decltype(I)::value;
    static_for<4>([&](auto G) { constexpr int gq = decltype(G)::value; if constexpr (i == wait_at(gq)) { retry6(IC<4 + 6 * gq>{}); STAMP(18 + 2 * gq); } });
    if constexpr (i == SEND_W) red_write(redP, acc2[0]);
    if constexpr (i == SEND_R) {
      lds_bar();
      _Pragma("unroll") for (int j = 0; j < 2; ++j) {
        const f32x4 o = *(const f32x4*)(redP + (w ^ 4) * 2048 + j * 1024);
        _Pragma("unroll") for (int e = 0; e < 4; ++e)
            pout[j][e] = __builtin_amdgcn_perm(__float_as_uint(acc2[0][4 * j + e] + o[e]), (unsigned)(tag >> (32 * j)), 0x07060500u + e);
      }
      STAMP(25);
    }
    SB();
    emit(IC<MF(i)>{});
    rd2(IC<i + 2>{});
    SB();
    acc2[item(i).tile] = MFMA(w2f[i % 3], __builtin_bit_cast(bf16x8, hf[item(i).frag]), acc2[item(i).tile]);
    SB();
    static_for<4>([&](auto G) {
      constexpr int gq = decltype(G)::value, f0 = 4 + 6 * gq;
      if constexpr (i == wait_at(gq) - 2) {
        if constexpr (gq) STAMP(17 + 2 * gq);
        wait_vm<vm_for(L_(f0 + 2), MF(i + 1))>();
        _Pragma("unroll") for (int j = 0; j < 3; ++j) pin(hf[f0 + j]);
        chk = umax3(max8(hf[f0], hf[f0 + 1]), max4(hf[f0 + 2]), 0u); pin(chk);
      }
      if constexpr (i == wait_at(gq) - 1) {
        wait_vm<vm_for(L_(f0 + 5), MF(i + 1))>();
        _Pragma("unroll") for (int j = 3; j < 6; ++j) pin(hf[f0 + j]);
        chk = umax3(max8(hf[f0 + 3], hf[f0 + 4]), max4(hf[f0 + 5]), chk); pin(chk);
      }
      SB();
    });
  });
  STAMP(26);
  emit(IC<MF(56)>{});
  red_write(redO, acc2[1]);
  lds_bar();
  emit(IC<MF(57)>{});
  {
    const f32x4 o0 = *(const f32x4*)(redO + (w ^ 4) * 2048), o1 = *(const f32x4*)(redO + (w ^ 4) * 2048 + 1024);
    float s[8];
    _Pragma("unroll") for (int e = 0; e < 8; ++e) { s[e] = (e < 4 ? o0[e] : o1[e - 4]) + acc2[1][e]; pin(s[e]); }
    STAMP(27);
    u32x4 p0, p1;
    {
      unsigned t0, t1, left = SPIN_CAP; u64 m;
#define CHK4(a, b, c, d, dst, tg) "v_perm_b32 %[t0], v" #b ", v" #a ", %[s1]\n\tv_perm_b32 %[t1], v" #d ", v" #c ", %[s1]\n\tv_perm_b32 %[t0], %[t1], %[t0], %[s2]\n\tv_cmp_ne_u32 " dst ", " tg ", %[t0]\n\t"
#define POLL(a, b, c, d, e, f, g, h, lab) "s_waitcnt vmcnt(%[vm])\n\t" CHK4(a, b, c, d, "vcc", "%[lo]") CHK4(e, f, g, h, "%[m]", "%[hi]") \
      "s_or_b64 vcc, vcc, %[m]\n\ts_cbranch_scc0 " lab "\n\t" \
      "buffer_load_dwordx4 v[" #a ":" #d "], %[vo], %[rs], %[o0] offen sc1\n\tbuffer_load_dwordx4 v[" #e ":" #h "], %[vo], %[rs], %[o1] offen sc1\n\ts_sub_i32 %[n], %[n], 1\n\t"
#define TAKE(a, b, c, d, e, f, g, h) "v_and_b32 v232, %[mk], v" #a "\n\tv_and_b32 v233, %[mk], v" #b "\n\tv_and_b32 v234, %[mk], v" #c "\n\tv_and_b32 v235, %[mk], v" #d "\n\t" \
      "v_and_b32 v236, %[mk], v" #e "\n\tv_and_b32 v237, %[mk], v" #f "\n\tv_and_b32 v238, %[mk], v" #g "\n\tv_and_b32 v239, %[mk], v" #h "\n\t"
#define POLLS POLL(208, 209, 210, 211, 212, 213, 214, 215, "2f")
      asm volatile("1:\n\t" POLLS
                   "s_cmp_gt_i32 %[n], 0\n\ts_cbranch_scc1 1b\n\ts_waitcnt vmcnt(0)\n"
                   "2:\n\t" TAKE(208, 209, 210, 211, 212, 213, 214, 215) "\n5:"
                   : "={v[232:235]}"(p0), "={v[236:239]}"(p1), [t0] "=&v"(t0), [t1] "=&v"(t1), [m] "=&s"(m), [n] "+s"(left),
                     "+{v[208:211]}"(pin_[0]), "+{v[212:215]}"(pin_[1])
                   : [s1] "s"(0x0c0c0400u), [s2] "s"(0x05040100u), [lo] "s"((unsigned)tag), [hi] "s"((unsigned)(tag >> 32)), [mk] "s"(0xffffff00u),
                     [vo] "v"(vl16), [rs] "s"(rh), [o0] "s"(sp_in), [o1] "s"(sp_in + SO), [vm] "i"(2 * NPOLL - 2)
                   : "vcc", "scc", "memory");
    }
    STAMP(29);
    u32x4 o;
    _Pragma("unroll") for (int e = 0; e < 4; ++e)
        o[e] = pack2(s[2 * e] + __uint_as_float((e < 2 ? p0 : p1)[(2 * e) & 3]), s[2 * e + 1] + __uint_as_float((e < 2 ? p0 : p1)[(2 * e + 1) & 3]));
    *(u32x4*)(out + vout) = o;
    SB();
    asm volatile("" ::"{v[208:211]}"(pin_[0]), "{v[212:215]}"(pin_[1]));
  }
  asm volatile("s_endpgm" ::: "memory");
}
}

extern "C" void kernel_launch(void* const* d_in, const int* in_sizes, int n_in, void* d_out, int out_size, void* d_ws, size_t ws_size,
                              hipStream_t stream) {
  ffn::ffn_kernel<<<256, 512, 0, stream>>>((const char*)d_in[0], (const char*)d_in[1], (const char*)d_in[2], (const char*)d_in[3],
                                           (const char*)d_in[4], (char*)d_out, (char*)d_ws);
}
```

```cpp
#include <hip/hip_runtime.h>
#include <stdint.h>
#include <utility>

typedef short bf16x8 __attribute__((ext_vector_type(8)));
typedef short s16x4 __attribute__((ext_vector_type(4)));
typedef float f32x16 __attribute__((ext_vector_type(16)));
typedef float f32x4 __attribute__((ext_vector_type(4)));
typedef float f32x2 __attribute__((ext_vector_type(2)));
typedef __bf16 bf16x2_t __attribute__((ext_vector_type(2)));
typedef unsigned u32x4 __attribute__((ext_vector_type(4)));
typedef unsigned u32x8 __attribute__((ext_vector_type(8)));
typedef unsigned long long u64;
#define LAS __attribute__((address_space(3)))
#define GAS __attribute__((address_space(1)))
#define CAS __attribute__((address_space(4)))
#define DI __device__ __forceinline__

namespace ffn {
constexpr int M = 2048, D = 448, F = 1792;
constexpr int NWORK = 224;
constexpr int NSLOT = 20, NUNIT = 42, UNIT = 8192;
constexpr unsigned CTRL_OFF = 12u << 20;
constexpr unsigned FLAG_OFF = CTRL_OFF + 4096;
constexpr unsigned HU_OFF = CTRL_OFF + 65536;
constexpr unsigned WS_BYTES = 13u << 20;
constexpr unsigned SENT = 0xFFFFFFFFu;
constexpr unsigned SO = 1835008, SG = 8192, SF = 131072, SW = 1024;
static_assert(5 * SO + 15 * SG + 13 * SF + 7 * SW + 1024 <= CTRL_OFF, "layout");
constexpr int CODE_LINES = 108;
constexpr int SPIN_CAP = 1 << 16;

enum { K_D = 0, K_HS, K_P, K_FL, K_L };
constexpr int OP(int k, int i) { return k * 64 + i; }
constexpr int PRO = 0, FIRST = 7;
constexpr int TOP(int s) { return 8 + 8 * s; }
constexpr int ST(int s, int k) { return 8 + 8 * s + 1 + k; }
constexpr int PB(int m) { return ST(7 + m / 4, m % 4); }
constexpr int GB(int j) { return 128 + j; }
constexpr int MF(int i) { return 160 + i; }
constexpr int END = 230;
struct Ent { int op, pos; };
#define D_(u) OP(K_D, u)
#define L_(f) OP(K_L, f)
#define P_(i) OP(K_P, i)
#define L6(f, p0, p1, p2, p3, p4, p5) {L_(f), p0}, {L_(f + 1), p1}, {L_(f + 2), p2}, {L_(f + 3), p3}, {L_(f + 4), p4}, {L_(f + 5), p5}
#define L6X(f, ...) L6(f, __VA_ARGS__)
#define GA_M 12
#define GA_E 25
#define FILLS 4, 4, 4, 2
#define P_FL0 ST(12, 1)
#define P_FL1 ST(13, 2)
#define P_L0 ST(13, 1), ST(13, 3), GB(0), GB(1), GB(1), GB(2)
#define P_L1 GB(6), GB(7), MF(0), MF(1), MF(2), MF(3)
#define M1(i) MF(i), MF(i + 1), MF(i + 2), MF(i + 3), MF(i + 4), MF(i + 5)
#define P_L2 M1(8)
#define P_L3 M1(18)
#define RD_LEAD 4
#define PROBES 50, 53
#define PRIAT 0, 18, 34
#define HPOS 2
constexpr int NP = 2;
#define W2BAR 5
#define BARS 1, 1, 1, 1, 1, 1, 1, 1, 1, 1, 0, 0, 1, 0
constexpr int ga_at(int j) { return j <= 8 ? j * GA_M / 8 : GA_M + (j - 8) * (GA_E - GA_M) / 8; }
constexpr int FILL[4] = {FILLS};
constexpr int wait_at(int g) { int m = 4; for (int i = 0; i <= g; ++i) m += FILL[i] + (i ? 6 : 0); return m; }
constexpr int SEND = wait_at(3) + 6, SPLIT = SEND - RD_LEAD;
constexpr int PRB[2] = {PROBES}, PRA[3] = {PRIAT};
constexpr int PUB = 9;
constexpr Ent VRAW[] = {
  {D_(0), PRO}, {D_(1), PRO}, {D_(2), PRO}, {D_(3), PRO + 1}, {D_(4), PRO + 1}, {D_(5), PRO + 1},
  {D_(6), FIRST}, {D_(7), FIRST}, {D_(8), FIRST},
  {D_(9), ST(0, 0)}, {D_(10), ST(0, 1)}, {D_(11), ST(0, 2)}, {D_(12), ST(0, 3)},
  {D_(13), ST(1, 0)}, {D_(14), ST(1, 1)}, {D_(15), ST(1, 2)}, {D_(16), ST(1, 3)},
  {D_(17), ST(2, 0)}, {D_(18), ST(2, 1)}, {D_(19), ST(2, 2)}, {D_(20), ST(2, 3)},
  {D_(21), ST(3, 0)}, {D_(22), ST(3, 1)}, {P_(0), ST(3, 3)},
  {D_(23), ST(4, 0)}, {D_(24), ST(4, 2)},
  {D_(25), ST(5, 0)}, {P_(1), ST(5, 1)}, {D_(26), ST(5, 3)},
  {D_(27), ST(6, 0)}, {D_(28), ST(6, 2)},
  {D_(29), ST(7, 0)}, {D_(30), ST(7, 1)}, {D_(31), ST(7, 3)},
  {D_(32), ST(8, 0)}, {D_(33), ST(8, 1)}, {D_(34), ST(8, 3)},
  {D_(35), ST(9, 0)}, {D_(36), ST(9, 1)}, {D_(37), ST(9, 3)},
  {D_(38), ST(10, 2)}, {D_(39), ST(10, 3)},
  {D_(40), ST(11, 0)}, {D_(41), ST(11, 2)},
  {OP(K_FL, 0), P_FL0},
  {OP(K_HS, 0), PB(ga_at(8))}, {OP(K_HS, 1), PB(ga_at(16))},
  L6X(4, P_L0),
  {OP(K_FL, 1), P_FL1},
  {OP(K_HS, 2), GB(3)},
  {OP(K_HS, 3), GB(7)},
  L6X(10, P_L1),
  L6X(16, P_L2),
  L6X(22, P_L3),
};
constexpr int NV = sizeof(VRAW) / sizeof(VRAW[0]);
struct Prog { Ent e[NV]; constexpr const Ent& operator[](int i) const { return e[i]; } };
constexpr Prog sort_prog() {
  Prog p{}; int n = 0;
  for (int pos = 0; pos <= END; ++pos) for (int i = 0; i < NV; ++i) if (VRAW[i].pos == pos) p.e[n++] = VRAW[i];
  return p;
}
constexpr Prog VSEQ = sort_prog();
constexpr bool BAR[14] = {BARS};
constexpr int idx_of(int op) { for (int i = 0; i < NV; ++i) if (VSEQ[i].op == op) return i; return -1000; }
constexpr int pos_of(int op) { return VSEQ[idx_of(op)].pos; }
constexpr int cnt_before(int pos) { int n = 0; for (int i = 0; i < NV; ++i) if (VSEQ[i].pos < pos) ++n; return n; }
constexpr int vm_for(int op, int pos) { return cnt_before(pos) - 1 - idx_of(op); }
constexpr int slot_of(int u) { return u % NSLOT; }
constexpr int last_unit_of_reads(int s) { return s < 7 ? 3 * s + 2 : 21 + (s - 7); }
constexpr int use_step(int u) { return u < 21 ? u / 3 : u < 28 ? u - 21 + 7 : 99; }
constexpr int next_bar(int s) { int t = s + 1; while (t < 14 && !BAR[t]) ++t; return t; }
constexpr int top_need(int s) {
  int nb = next_bar(s), last = nb < 14 ? nb : 13, m = -1;
  for (int c = s + 1; c <= last; ++c) for (int u = (c < 7 ? 3 * c : 21 + c - 7); u <= last_unit_of_reads(c); ++u) if (idx_of(D_(u)) > m) m = idx_of(D_(u));
  for (int i = 0; i < NP; ++i) if (s >= PUB && idx_of(P_(i)) > m) m = idx_of(P_(i));
  return m;
}
constexpr int last_w2() { int m = 28; for (int u = 28; u < 42; ++u) if (idx_of(D_(u)) > idx_of(D_(m))) m = u; return m; }
constexpr int flag_user(int st) { return st ? OP(K_HS, 2) : L_(4); }
constexpr bool check_prog() {
  for (int u = NSLOT; u < NUNIT; ++u) {
    int us = use_step(u - NSLOT), p = pos_of(D_(u)); bool ok = false;
    for (int t = us + 1; t < 14; ++t) if (BAR[t] && TOP(t) <= p) ok = true;
    if (!ok) return false;
  }
  for (int s = 0; s < 14; ++s) if (BAR[s] && top_need(s) >= cnt_before(TOP(s))) return false;
  if (!BAR[0] || !BAR[1] || ga_at(16) > 27 || W2BAR > 6) return false;
  for (int u = 28; u < 42; ++u) if (pos_of(D_(u)) >= GB(W2BAR)) return false;
  for (int st = 0; st < 2; ++st) {
    if (idx_of(flag_user(st)) < idx_of(OP(K_FL, st)) || idx_of(L_(4 + 12 * st)) < idx_of(flag_user(st))) return false;
    if (!st && (!BAR[PUB] || pos_of(OP(K_FL, st)) < TOP(PUB) || pos_of(OP(K_HS, 0)) < TOP(PUB))) return false;
  }
  for (int i = 0; i < 4; ++i) if ((i < NP && idx_of(P_(i)) > idx_of(OP(K_HS, i))) || (i && idx_of(OP(K_HS, i)) < idx_of(OP(K_HS, i - 1)))) return false;
  for (int f = 5; f < 28; ++f) if (idx_of(L_(f)) < idx_of(L_(f - 1))) return false;
  for (int g = 0, o = 0; g < 4; ++g) { o += FILL[g]; if (pos_of(L_(9 + 6 * g)) >= MF(wait_at(g) - 1) || o > 4 + 6 * g) return false; }
  if (SEND + 3 > 55 || PRB[0] < SPLIT || PRB[1] < PRB[0] || PRB[1] > 56 || SPLIT < wait_at(3)) return false;
  return true;
}
static_assert(check_prog(), "schedule");
struct Item { int tile, frag; };
constexpr Item item(int i) {
  int p = 0, o = 0;
  for (int n = 0;; ++n) {
    bool own = p >= 28;
    for (int g = 0; g < 4; ++g) if (p == 4 + 6 * g && n < wait_at(g)) own = true;
    if (n == i) return Item{own, own ? o : p};
    if (own) ++o; else ++p;
  }
}
constexpr int frag_j(int f) { return f < 4 ? 0 : 1 + (f - 4) % 6; }
constexpr int frag_q(int f) { return f < 4 ? f : (f - 4) / 6; }
constexpr int clampv(int n) { return n > 63 ? 63 : n; }

template <int N> DI void wait_vm() { static_assert(N >= 0, "neg"); asm volatile("s_waitcnt vmcnt(%0)" ::"i"(clampv(N)) : "memory"); }
#define SB() __builtin_amdgcn_sched_barrier(0)
template <int N> DI void vm_bar() {
  static_assert(N >= 0, "neg");
  SB(); asm volatile("s_waitcnt vmcnt(%0)\n\ts_barrier" ::"i"(clampv(N)) : "memory"); SB();
}
DI void lds_bar() {
  SB(); __builtin_amdgcn_s_waitcnt(0xC07F); asm volatile("s_waitcnt lgkmcnt(0)\n\ts_barrier" ::: "memory"); SB();
}
template <class T> DI void pin(T& x) { asm volatile("" : "+v"(x)); }
template <class T> DI void pins(T& x) { asm volatile("" : "+s"(x)); }

template <int OFF> DI void dma(u32x4 rsrc, unsigned voff, unsigned soff, unsigned ldsw) {
  asm volatile("s_add_u32 m0, %3, %4\n\ts_nop 0\n\tbuffer_load_dwordx4 %0, %1, %2 offen lds" ::"v"(voff), "s"(rsrc), "s"(soff), "s"(ldsw), "i"(OFF) : "memory", "scc");
}
DI u32x4 make_rsrc(const void* p, unsigned bytes) {
  u64 a = (u64)p; u32x4 r; r.x = (unsigned)a; r.y = (unsigned)(a >> 32); r.z = bytes; r.w = 0x00020000u; return r;
}
DI void st_sc1(u32x4 v, u32x4 rsrc, unsigned voff, unsigned soff) {
  asm volatile("buffer_store_dwordx4 %0, %1, %2, %3 offen sc1\n\ts_nop 1" ::"v"(v), "v"(voff), "s"(rsrc), "s"(soff) : "memory");
}
DI void ld_sc1(u32x4& a, u32x4 rsrc, unsigned voff, unsigned soff) {
  asm volatile("buffer_load_dwordx4 %0, %1, %2, %3 offen sc1" : "=&v"(a) : "v"(voff), "s"(rsrc), "s"(soff) : "memory");
}
template <int OFF> DI void st_sc1_at(u32x4 v, u32x4 rsrc, unsigned voff, unsigned soff) {
  asm volatile("buffer_store_dwordx4 %0, %1, %2, %3 offen offset:%4 sc1\n\ts_nop 1" ::"v"(v), "v"(voff), "s"(rsrc), "s"(soff), "i"(OFF) : "memory");
}
template <int I> DI void probe(u64& p, u32x4 rsrc, unsigned soff) {
  if constexpr (I == 0) asm volatile("v_mov_b32 v224, 0\n\tv_mov_b32 v225, 0\n\ts_mov_b64 exec, 1\n\tbuffer_atomic_or v224, off, %1, %2 sc0 sc1\n\ts_mov_b64 exec, -1" : "={v[224:225]}"(p) : "s"(rsrc), "s"(soff) : "memory");
  else asm volatile("s_mov_b64 exec, 1\n\tbuffer_atomic_or v225, off, %1, %2 sc0 sc1\n\ts_mov_b64 exec, -1" : "+{v[224:225]}"(p) : "s"(rsrc), "s"(soff) : "memory");
}
DI void hint_st(unsigned tag, u32x4 rsrc, unsigned soff) {
  asm volatile("s_mov_b64 exec, 1\n\tbuffer_store_dword %0, off, %1, %2 sc1\n\ts_mov_b64 exec, -1\n\ts_nop 0" ::"v"(tag), "s"(rsrc), "s"(soff) : "memory");
}
#define LOOK(r) "s_waitcnt vmcnt(1)\n\tv_readfirstlane_b32 %[t], v" #r "\n\ts_cmp_eq_u32 %[t], %[lo]\n\ts_cbranch_scc1 2f\n\t" \
                "v_mov_b32 v" #r ", 0\n\ts_mov_b64 exec, 1\n\tbuffer_atomic_or v" #r ", off, %[rs], %[hu] sc0 sc1\n\ts_mov_b64 exec, -1\n\t"
#define FETCH "buffer_load_dwordx4 v[208:211], %[vo], %[rs], %[o0] offen sc1\n\tbuffer_load_dwordx4 v[212:215], %[vo], %[rs], %[o0] offen offset:1024 sc1\n\t" \
              "buffer_load_dwordx4 v[216:219], %[vo], %[rs], %[o0] offen offset:2048 sc1\n\tbuffer_load_dwordx4 v[220:223], %[vo], %[rs], %[o0] offen offset:3072 sc1\n\t"
DI void fetch_when_told(unsigned& left, u64& p, u32x4 (&a)[4], unsigned lo, unsigned voff, u32x4 rsrc, unsigned hu, unsigned o0) {
  unsigned t;
  asm volatile("1:\n\t" LOOK(224) LOOK(225) "s_sub_i32 %[n], %[n], 1\n\ts_cmp_gt_i32 %[n], 0\n\ts_cbranch_scc1 1b\n2:\n\t" FETCH
               : [t] "=&s"(t), [n] "+s"(left), "+{v[224:225]}"(p), "={v[208:211]}"(a[0]), "={v[212:215]}"(a[1]), "={v[216:219]}"(a[2]), "={v[220:223]}"(a[3])
               : [lo] "s"(lo), [vo] "v"(voff), [rs] "s"(rsrc), [hu] "s"(hu), [o0] "s"(o0) : "scc", "memory");
}
#define CHK4(a, b, c, d, dst, tg) "v_perm_b32 %[t0], v" #b ", v" #a ", %[s1]\n\tv_perm_b32 %[t1], v" #d ", v" #c ", %[s1]\n\tv_perm_b32 %[t0], %[t1], %[t0], %[s2]\n\tv_cmp_ne_u32 " dst ", " tg ", %[t0]\n\t"
DI void until_tagged(unsigned& left, u64& p, u32x4 (&a)[4], u64 tag, unsigned voff, u32x4 rsrc, unsigned o0) {
  unsigned t0, t1; u64 m0, m1, m2;
  asm volatile("1:\n\ts_waitcnt vmcnt(0)\n\t" CHK4(208, 209, 210, 211, "vcc", "%[lo]") CHK4(212, 213, 214, 215, "%[m0]", "%[hi]") CHK4(216, 217, 218, 219, "%[m1]", "%[lo]") CHK4(220, 221, 222, 223, "%[m2]", "%[hi]")
               "s_or_b64 vcc, vcc, %[m0]\n\ts_or_b64 %[m1], %[m1], %[m2]\n\ts_or_b64 vcc, vcc, %[m1]\n\ts_cbranch_scc0 2f\n\t" FETCH
               "s_sub_i32 %[n], %[n], 1\n\ts_cmp_gt_i32 %[n], 0\n\ts_cbranch_scc1 1b\n\ts_waitcnt vmcnt(0)\n2:"
               : [t0] "=&v"(t0), [t1] "=&v"(t1), [m0] "=&s"(m0), [m1] "=&s"(m1), [m2] "=&s"(m2), [n] "+s"(left), "+{v[224:225]}"(p),
                 "+{v[208:211]}"(a[0]), "+{v[212:215]}"(a[1]), "+{v[216:219]}"(a[2]), "+{v[220:223]}"(a[3])
               : [s1] "s"(0x0c0c0400u), [s2] "s"(0x05040100u), [lo] "s"((unsigned)tag), [hi] "s"((unsigned)(tag >> 32)), [vo] "v"(voff), [rs] "s"(rsrc), [o0] "s"(o0)
               : "vcc", "scc", "memory");
}
DI void flag_st(u64 tag, u32x4 rsrc, unsigned soff) {
  asm volatile("s_mov_b64 exec, 1\n\tbuffer_store_dwordx2 %0, off, %1, %2 sc1\n\ts_mov_b64 exec, -1\n\ts_nop 0" ::"v"(tag), "s"(rsrc), "s"(soff) : "memory");
}
template <int MASK> DI void flag_ld(u64& f, u32x4 rsrc, unsigned voff, unsigned soff) {
  asm volatile("s_mov_b64 exec, %4\n\tbuffer_load_dwordx2 %0, %1, %2, %3 offen sc1\n\ts_mov_b64 exec, -1" : "+v"(f) : "v"(voff), "s"(rsrc), "s"(soff), "i"(MASK) : "memory");
}
DI void touch(unsigned& t, const char* p) { asm volatile("global_load_dword %0, %1, off" : "=&v"(t) : "v"(p) : "memory"); }
DI u64 mix64(u64 z) { z += 0x9E3779B97F4A7C15ull; z = (z ^ (z >> 30)) * 0xBF58476D1CE4E5B9ull; z = (z ^ (z >> 27)) * 0x94D049BB133111EBull; return z ^ (z >> 31); }
constexpr float GC0 = -2.0f * 0.7978845608028654f * 1.4426950408889634f, GC1 = GC0 * 0.044715f;
DI unsigned pack2(float a, float b) { f32x2 v = {a, b}; return __builtin_bit_cast(unsigned, __builtin_convertvector(v, bf16x2_t)); }
DI unsigned nosent(unsigned p) { return p < SENT ? p : SENT - 1; }
DI unsigned umax3(unsigned a, unsigned b, unsigned c) { unsigned m = a > b ? a : b; return m > c ? m : c; }
DI unsigned max8(u32x4 a, u32x4 b) { return umax3(umax3(a.x, a.y, a.z), umax3(a.w, b.x, b.y), b.z > b.w ? b.z : b.w); }
DI unsigned max4(u32x4 a) { return umax3(umax3(a.x, a.y, a.z), a.w, 0u); }
DI bool any_sent(unsigned m) { return __builtin_amdgcn_ballot_w64(m == SENT) != 0; }
DI unsigned gelu2pk(float a, float b) {
  const f32x2 v = {a, b}, c0 = {GC0, GC0}, c1 = {GC1, GC1}, one = {1.f, 1.f};
  const f32x2 z = v * __builtin_elementwise_fma(v * v, c1, c0);
  const f32x2 e = {__builtin_amdgcn_exp2f(z.x), __builtin_amdgcn_exp2f(z.y)}, d = e + one;
  const f32x2 rc = {__builtin_amdgcn_rcpf(d.x), __builtin_amdgcn_rcpf(d.y)};
  unsigned p = nosent(__builtin_bit_cast(unsigned, __builtin_convertvector(v * rc, bf16x2_t)));
  asm volatile("" : "+v"(p));
  return p;
}
DI float bflo(unsigned d) { return __uint_as_float(d << 16); }
DI float bfhi(unsigned d) { return __uint_as_float(d & 0xffff0000u); }
template <class Fn, int... I> DI void static_for_impl(Fn&& f, std::integer_sequence<int, I...>) { (f(std::integral_constant<int, I>{}), ...); }
template <int N, class Fn> DI void static_for(Fn&& f) { static_for_impl(f, std::make_integer_sequence<int, N>{}); }
template <int V> using IC = std::integral_constant<int, V>;
#define MFMA(a, b, c) __builtin_amdgcn_mfma_f32_32x32x16_bf16((a), (b), (c), 0, 0, 0)

__global__ __launch_bounds__(512, 2) void ffn_kernel(const char* x, const char* w1, const char* __restrict__ b1,
                                                     const char* w2, const char* __restrict__ b2, char* __restrict__ out, char* ws) {
  __shared__ __attribute__((aligned(16))) char lds[NSLOT * UNIT];
  const unsigned b = blockIdx.x, tid = threadIdx.x;
  u64 pc; unsigned tc;
  asm volatile("s_cmp_lt_u32 %[b], %[n]\n\ts_cbranch_scc0 1f\n\t"
               "s_getpc_b64 %[pc]\n\tv_lshlrev_b32 %[tc], 5, %[tid]\n\tv_min_u32 %[tc], %[last], %[tc]\n\tglobal_load_dword %[tc], %[tc], %[pc]\n1:"
               : [pc] "=&s"(pc), [tc] "=&v"(tc) : [b] "s"(b), [n] "i"(8), [tid] "v"(tid), [last] "i"((CODE_LINES - 1) * 128) : "scc", "memory");
  if (b >= NWORK) {
    const int hj = b - NWORK;
    const int l = tid & 63, w = __builtin_amdgcn_readfirstlane(tid >> 6);
    GAS u64* const ctrl = (GAS u64*)(ws + CTRL_OFF);
    {
      unsigned t0, t1;
      auto a2 = [&](int n, int hf) { return w2 + (n / 448) * 114688 + hf * 57344 + (n % 448) * 128; };
      const int n = 196 * hj + (tid < 196 ? tid : tid < 392 ? tid - 196 : 0);
      touch(t0, tid < 196 ? w1 + n * 256 + 128 : tid < 392 ? a2(n, 0) : a2(196 * hj + tid - 392, 1));
      t1 = 0;
      if (w < 2) touch(t1, a2(196 * hj + 120 + (tid < 76 ? tid : 0), 1));
      {
        const u64 tag = mix64(*(const CAS u64*)(ws + CTRL_OFF));
        const int set = (hj & 6) * 4 + (hj >> 3) * 2 + (hj & 1);
        const u32x4 rh = make_rsrc(ws, WS_BYTES), sent = {SENT, SENT, SENT, SENT};
        const unsigned base = 2 * SO + (set >> 1) * SG + 7 * (set & 1) * SF + w * SW;
        _Pragma("unroll") for (int i = 0; i < 14; ++i) st_sc1(sent, rh, l * 16, base + (i / 7) * SO + (i % 7) * SF);
        asm volatile("s_waitcnt vmcnt(0)\n\ts_barrier" ::: "memory");
        u64 tv = tag; pin(tv);
        if (w == 0) flag_st(tv, rh, FLAG_OFF + set * 256 + 128);
      }
      asm volatile("s_waitcnt vmcnt(0)" ::"v"(t0), "v"(t1), "v"(tc) : "memory");
    }
    if (hj >= 1 && hj <= 4 && w == 0 && (hj - 1) * 64 + l < NWORK)
      asm volatile("global_atomic_or %0, %1, %2 sc1" ::"v"(HU_OFF + ((hj - 1) * 64 + l) * 128), "v"(0u), "s"(ws) : "memory");
    if (hj == 0 && w == 7) {
      const u64 e = __hip_atomic_load(ctrl, __ATOMIC_RELAXED, __HIP_MEMORY_SCOPE_AGENT), tag = mix64(e);
      for (int spin = 0; spin < SPIN_CAP; ++spin) {
        bool ok = true;
        _Pragma("unroll") for (int i = 0; i < 4; ++i) {
          const int j = l + 64 * i;
          ok &= __hip_atomic_load((GAS u64*)(ws + FLAG_OFF + (j >> 3) * 256 + ((j & 7) == 7 ? 128 : (j & 7) * 8)), __ATOMIC_RELAXED, __HIP_MEMORY_SCOPE_AGENT) == tag;
        }
        if (__builtin_amdgcn_ballot_w64(!ok) == 0) break;
        __builtin_amdgcn_s_sleep(16);
      }
      if (l == 0) __hip_atomic_store(ctrl, e + 1, __ATOMIC_RELAXED, __HIP_MEMORY_SCOPE_AGENT);
    }
    return;
  }
  const unsigned qd = ((b >> 3) * 37) >> 8; unsigned r = (b >> 3) - 7 * qd;
  auto mod7 = [](int v) { return v >= 7 ? v - 7 : v; };
  int w = __builtin_amdgcn_readfirstlane(tid) >> 6;
  int kh = b & 1, g = 4 * ((b >> 1) & 3) + qd;
  const int fs = 7 * kh + r, rot = mod7(r + 2 * qd);
  unsigned ldsw = (unsigned)(size_t)(LAS char*)lds + w * 1024;
  const unsigned prow = __umul24(tid >> 3, 896);
  unsigned vx = prow + (((tid ^ (tid >> 4)) & 7) << 4), vx2 = vx + 64 * 896;
  unsigned vw1 = 4 * prow + (((tid & 7) ^ ((tid >> 2) & 4)) << 4);
  const unsigned sx0 = g * 128 * 896; unsigned s10 = fs * 256;
  unsigned o0 = sx0 + rot * 128, o2 = s10 + rot * (64 * 3584);
  pin(vx); pin(vx2); pin(vw1); pins(ldsw); pins(o0); pins(o2);
  SB();
  pins(x); pins(w1);
  const u32x4 rx = make_rsrc(x, M * D * 2), r1 = make_rsrc(w1, D * F * 2);
  dma<0>(rx, vx, o0, ldsw); dma<UNIT>(rx, vx2, o0, ldsw); dma<2 * UNIT>(r1, vw1, o2, ldsw);
  SB();
  pins(w); pins(r); pins(kh); pins(g); pins(s10);
  const int mt = w & 3, ft = w >> 2;
  const unsigned ob1 = s10 + 64 * ft, ob2 = (64 * r + 32 * kh) * 2;
  const CAS u32x8* b1p = (const CAS u32x8*)(b1 + ob1);
  const u32x8 b1a0 = b1p[0], b1a1 = b1p[1], b1b0 = b1p[4], b1b1 = b1p[5];
  const CAS u32x8* b2p = (const CAS u32x8*)(b2 + ob2);
  const u32x8 b2s0 = b2p[0], b2s1 = b2p[1];
  u64 epoch = *(const CAS u64*)(ws + CTRL_OFF);
  SB();
  {
    const int step = rot == 6 ? -6 : 1;
    unsigned o3 = o0 + step * 128, o5 = o2 + step * (64 * 3584);
    pins(o3); pins(o5);
    asm volatile("s_barrier" ::: "memory");
    dma<3 * UNIT>(rx, vx, o3, ldsw); dma<4 * UNIT>(rx, vx2, o3, ldsw); dma<5 * UNIT>(r1, vw1, o5, ldsw);
    SB();
  }
  static_assert(pos_of(D_(0)) == PRO && pos_of(D_(5)) == PRO + 1 && cnt_before(PRO + 2) == 6 && UNIT == 0x2000, "prologue");
  const int l = tid & 63;
  const u32x4 r2 = make_rsrc(w2, D * F * 2), rh = make_rsrc(ws, WS_BYTES);
  unsigned vw2, s20, vl16, vfl, sh_row, sh_own, sp_x, hu_x, sf_g, vout;

  u32x4 hf[28];
  u64 tag, tagv, flv[2];
  auto hoff = [&](auto Fr) { constexpr int f = decltype(Fr)::value; return sh_row + mod7(r + frag_j(f)) * SF + frag_q(f) * SO; };
  auto flag_load = [&](auto St) {
    constexpr int st = decltype(St)::value;
    flv[st] = tagv;
    flag_ld<(st ? 1 : 0x7f)>(flv[st], rh, vfl, sf_g + st * 128);
  };
  auto flag_check = [&](auto St, auto N) {
    constexpr int st = decltype(St)::value;
    wait_vm<decltype(N)::value>();
    pin(flv[st]);
    if (__builtin_expect(__builtin_amdgcn_ballot_w64(flv[st] != tag) != 0, 0)) {
      for (int spin = 0; spin < SPIN_CAP; ++spin) {
        flag_load(St);
        wait_vm<0>();
        pin(flv[st]);
        if (__builtin_amdgcn_ballot_w64(flv[st] != tag) == 0) break;
      }
    }
  };
  auto do_op = [&](auto Ix) {
    constexpr int ix = decltype(Ix)::value, op = VSEQ[ix].op, k = op / 64, i = op % 64;
    if constexpr (op == flag_user(0) || op == flag_user(1)) flag_check(IC<op == flag_user(1)>{}, IC<ix - 1 - idx_of(OP(K_FL, op == flag_user(1)))>{});
    if constexpr (k == K_D) {
      constexpr int dst = slot_of(i) * UNIT;
      if constexpr (i < 21) {
        constexpr int c = i / 3, kk = i % 3;
        if constexpr (kk < 2) dma<dst>(rx, kk ? vx2 : vx, sx0 + mod7(c + rot) * 128, ldsw);
        else dma<dst>(r1, vw1, s10 + mod7(c + rot) * (64 * 3584), ldsw);
      } else if constexpr (i < 28) {
        dma<dst>(r1, vw1, s10 + 128 + mod7(i - 21 + rot) * (64 * 3584), ldsw);
      } else {
        constexpr int j = (i - 28) % 7, half = (i - 28) / 7;
        dma<dst>(r2, vw2, s20 + mod7(r + j) * (128 * 896) + half * (64 * 896), ldsw);
      }
    } else if constexpr (k == K_HS) st_sc1(hf[i], rh, vl16, sh_own + i * SO);
    else if constexpr (k == K_P) {
      const u32x4 sent = {SENT, SENT, SENT, SENT};
      st_sc1(sent, rh, vl16, sh_own + i * SO);
    } else if constexpr (k == K_FL) flag_load(IC<i>{});
    else if constexpr (k == K_L) ld_sc1(hf[i], rh, vl16, hoff(IC<i>{}));
  };
  auto emit = [&](auto P) {
    constexpr int pos = decltype(P)::value;
    static_for<NV>([&](auto I) { if constexpr (VSEQ[decltype(I)::value].pos == pos) do_op(I); });
  };

  asm volatile("" ::"s"(w2), "s"(out), "s"(ws));
  int l2 = l; pin(l2);
  const int h = l2 >> 5;
  vw2 = (8 * w + (l2 >> 3)) * 896 + (((l2 & 7) ^ (((l2 >> 4) & 1) << 2)) << 4);
  vl16 = l2 * 16; vfl = l2 * 8;
  s20 = kh * 896 * 896 + r * 128;
  sh_row = g * SG + 7 * kh * SF + w * SW; sh_own = sh_row + r * SF;
  {
    const int xf = r + 7 * (kh ^ ft ^ 1);
    sp_x = (4 + (mt >> 1)) * SO + g * SG + xf * SF + (mt & 1) * 4096; hu_x = HU_OFF + (14 * g + xf) * 128 + mt * 4;
  }
  sf_g = FLAG_OFF + (2 * g + kh) * 256;
  vout = (128 * g + 32 * mt + (l2 & 31)) * 896 + (64 * r + 32 * kh + 8 * h) * 2;
  pin(vl16);
  SB();
  emit(IC<PRO + 2>{});
  SB();
  pin(vout); pin(vw2); pin(vfl);

  const int blk = (l2 >> 4) & 1, q = (l2 & 15) >> 2, p4 = l2 & 3;
  unsigned xrd[4], w1rd[3], w2rd[2][3];
  _Pragma("unroll") for (int s = 0; s < 4; ++s)
      xrd[s] = (mt >> 1) * UNIT + (32 * (mt & 1) + (l2 & 31)) * 128 + (((2 * s + h) ^ ((l2 >> 1) & 7)) << 4);
  w1rd[0] = (8 * h + q) * 128 + ((64 * ft + 32 * (p4 & 1) + 16 * blk + 8 * (p4 >> 1)) ^ ((q >> 1) << 6));
  w1rd[1] = w1rd[0] + 65536; w1rd[2] = w1rd[0] + 131072;
  pin(w1rd[0]); pin(w1rd[1]); pin(w1rd[2]);

  bf16x8 xf[28];
  bf16x8 wf[2][4];
  bf16x8 w2f[3];
  f32x16 acc1[2], acc2[2];
  float gprev, ge;
  _Pragma("unroll") for (int i = 0; i < 8; ++i) { const unsigned da = h ? b1a1[i] : b1a0[i]; acc1[0][2 * i] = bflo(da); acc1[0][2 * i + 1] = bfhi(da); }
  pin(acc1[0]);

  auto trd2 = [&](unsigned base, auto Off) {
    constexpr int off = decltype(Off)::value;
    s16x4 lo = __builtin_amdgcn_ds_read_tr16_b64_v4i16((LAS s16x4*)(lds + base + off)), hi = __builtin_amdgcn_ds_read_tr16_b64_v4i16((LAS s16x4*)(lds + base + off + 512));
    return __builtin_shufflevector(lo, hi, 0, 1, 2, 3, 4, 5, 6, 7);
  };
  auto reads = [&](auto S, auto K) {
    constexpr int s = decltype(S)::value, k = decltype(K)::value;
    constexpr int wo = slot_of(last_unit_of_reads(s)) * UNIT;
    if constexpr (s == 3 || s == 6) { xrd[k] += 65536; pin(xrd[k]); }
    if constexpr (s < 7) xf[4 * s + k] = *(const bf16x8*)(lds + xrd[k] + ((3 * s * UNIT) & 0xffff));
    wf[s & 1][k] = trd2(w1rd[wo >> 16], IC<(wo & 0xffff) + k * 2048>{});
  };
  auto rd2 = [&](auto I) {
    constexpr int i = decltype(I)::value;
    if constexpr (i < 56) {
      constexpr int f = item(i).frag, wo = slot_of(28 + 7 * (frag_q(f) >> 1) + frag_j(f)) * UNIT;
      w2f[i % 3] = trd2(w2rd[item(i).tile][wo >> 16], IC<(wo & 0xffff) + (frag_q(f) & 1) * 1024>{});
    }
  };

  __builtin_amdgcn_s_waitcnt(0xC07F);
  vm_bar<vm_for(D_(2), FIRST)>();
  asm volatile("" ::"v"(tc));
  emit(IC<FIRST>{});
  static_for<4>([&](auto K) { reads(IC<0>{}, K); });
  SB();
  _Pragma("unroll") for (int i = 0; i < 8; ++i) { const unsigned db = h ? b1b1[i] : b1b0[i]; acc1[1][2 * i] = bflo(db); acc1[1][2 * i + 1] = bfhi(db); }
  pin(acc1[1]);
  pins(epoch); tag = mix64(epoch); tagv = tag; pin(tagv);
  auto late_setup = [&]() {
    const unsigned keep = ft - 1;
    _Pragma("unroll") for (int e = 0; e < 8; ++e) {
      const unsigned d0 = (e < 4 ? b2s0[e] : b2s1[e - 4]) & keep, d1 = (e < 4 ? b2s0[e + 4] : b2s1[e]) & keep, d = h ? d1 : d0;
      acc2[1][2 * e] = bflo(d); acc2[1][2 * e + 1] = bfhi(d);
    }
    pin(acc2[1]);
    const unsigned t = (32 * ft + 16 * h + q) * 128 + ((16 * (p4 & 1) + 32 * blk + 8 * (p4 >> 1)) ^ ((q >> 1) << 6));
    _Pragma("unroll") for (int t3 = 0; t3 < 3; ++t3) { w2rd[0][t3] = (t ^ (64 * (1 - kh))) + t3 * 65536; w2rd[1][t3] = (t ^ (64 * kh)) + t3 * 65536; pin(w2rd[0][t3]); pin(w2rd[1][t3]); }
  };

  static_for<14>([&](auto S) {
    constexpr int s = decltype(S)::value, ps = s / 7, c = s % 7;
    if constexpr (BAR[s]) vm_bar<cnt_before(TOP(s)) - 1 - top_need(s)>(); else SB();
    if constexpr (s == PUB) { if (w == 0) flag_st(tagv, rh, sf_g + r * 8); }
    if constexpr (s == 12) *(volatile LAS unsigned*)(lds + 2 * UNIT + w * 4) = 0;
    emit(IC<TOP(s)>{});
    static_for<4>([&](auto K) {
      constexpr int k = decltype(K)::value;
      if constexpr (s < 13) { reads(IC<s + 1>{}, K); SB(); }
      acc1[ps] = MFMA(wf[s & 1][k], xf[4 * c + k], acc1[ps]);
      SB();
      static_for<17>([&](auto J) {
        constexpr int j = decltype(J)::value;
        if constexpr (s >= 7 && ga_at(j) == 4 * (s - 7) + k) {
          float en = 0.f;
          if constexpr (j < 16) { const float v = acc1[0][j]; en = __builtin_amdgcn_exp2f(v * __builtin_fmaf(v * v, GC1, GC0)); }
          if constexpr (j > 0) {
            float gv = acc1[0][j - 1] * __builtin_amdgcn_rcpf(1.0f + ge);
            if constexpr (j & 1) { gprev = gv; pin(gprev); }
            else { unsigned p = nosent(pack2(gprev, gv)); pin(p); hf[(j - 1) >> 3][((j - 1) >> 1) & 3] = p; }
          }
          ge = en; pin(ge);
        }
      });
      SB();
      emit(IC<ST(s, k)>{});
      SB();
      if constexpr (s == 2 && k == 3) { late_setup(); SB(); }
    });
  });
  static_for<8>([&](auto J) {
    constexpr int j = decltype(J)::value;
    if constexpr (j & 1) {
      hf[2 + (j >> 2)][(j & 3) - 1] = gelu2pk(acc1[1][2 * j - 2], acc1[1][2 * j - 1]);
      hf[2 + (j >> 2)][j & 3] = gelu2pk(acc1[1][2 * j], acc1[1][2 * j + 1]);
      SB();
    }
    if constexpr (j == W2BAR) vm_bar<vm_for(D_(last_w2()), GB(j))>();
    if constexpr (j >= 6) rd2(IC<j - 6>{});
    emit(IC<GB(j)>{});
    SB();
  });
  _Pragma("unroll") for (int i = 0; i < 16; ++i) acc2[0][i] = 0.f;
  unsigned chk;
  auto retry6 = [&](auto F0) {
    constexpr int f0 = decltype(F0)::value;
    if (__builtin_expect(any_sent(chk), 0)) {
      for (int spin = 0; spin < SPIN_CAP; ++spin) {
        static_for<6>([&](auto I) { ld_sc1(hf[f0 + decltype(I)::value], rh, vl16, hoff(IC<f0 + decltype(I)::value>{})); });
        wait_vm<0>();
        _Pragma("unroll") for (int i = 0; i < 6; ++i) pin(hf[f0 + i]);
        if (!any_sent(umax3(max8(hf[f0], hf[f0 + 1]), max8(hf[f0 + 2], hf[f0 + 3]), max8(hf[f0 + 4], hf[f0 + 5])))) break;
      }
    }
  };
  unsigned give = (4 + 2 * ft) * UNIT + mt * 4096 + vl16, take = (6 - 2 * ft) * UNIT + mt * 4096 + vl16;
  auto run_item = [&](auto I) {
    constexpr int i = decltype(I)::value;
    static_for<4>([&](auto G) { constexpr int gq = decltype(G)::value; if constexpr (i == wait_at(gq)) { retry6(IC<4 + 6 * gq>{}); } });
    SB();
    static_for<3>([&](auto Q) { if constexpr (i == PRA[decltype(Q)::value]) __builtin_amdgcn_s_setprio(3 - decltype(Q)::value); });
    if constexpr (i == wait_at(2) + 7) { pins(sp_x); pins(hu_x); pin(give); pin(take); }
    emit(IC<MF(i)>{});
    rd2(IC<i + 2>{});
    SB();
    acc2[item(i).tile] = MFMA(w2f[i % 3], __builtin_bit_cast(bf16x8, hf[item(i).frag]), acc2[item(i).tile]);
    SB();
    static_for<4>([&](auto G) {
      constexpr int gq = decltype(G)::value, f0 = 4 + 6 * gq;
      if constexpr (i == wait_at(gq) - 2) {
        wait_vm<vm_for(L_(f0 + 2), MF(i + 1))>();
        _Pragma("unroll") for (int j = 0; j < 3; ++j) pin(hf[f0 + j]);
        chk = umax3(max8(hf[f0], hf[f0 + 1]), max4(hf[f0 + 2]), 0u); pin(chk);
      }
      if constexpr (i == wait_at(gq) - 1) {
        wait_vm<vm_for(L_(f0 + 5), MF(i + 1))>();
        _Pragma("unroll") for (int j = 3; j < 6; ++j) pin(hf[f0 + j]);
        chk = umax3(max8(hf[f0 + 3], hf[f0 + 4]), max4(hf[f0 + 5]), chk); pin(chk);
      }
      SB();
    });
  };
  static_for<SPLIT>(run_item);
  f32x4 sib[4]; unsigned there;
  auto hand_over = [&](const f32x16& a) {
    _Pragma("unroll") for (int j = 0; j < 4; ++j) {
      f32x4 v; _Pragma("unroll") for (int e = 0; e < 4; ++e) v[e] = a[4 * j + e];
      *(f32x4*)(lds + give + j * 1024) = v;
    }
    SB(); __builtin_amdgcn_s_waitcnt(0xC07F); asm volatile("" ::: "memory");
    *(volatile LAS unsigned*)(lds + 2 * UNIT + w * 4) = 1;
    SB();
  };
  auto ask = [&]() {
    there = *(volatile LAS unsigned*)(lds + 2 * UNIT + (w ^ 4) * 4);
    SB(); asm volatile("" ::: "memory");
    _Pragma("unroll") for (int j = 0; j < 4; ++j) sib[j] = *(const f32x4*)(lds + take + j * 1024);
    SB();
  };
  auto got = [&]() {
    if (__builtin_expect(__builtin_amdgcn_readfirstlane(there) == 0, 0))
      for (int spin = 0; spin < SPIN_CAP; ++spin) { ask(); if (__builtin_amdgcn_readfirstlane(there) != 0) break; }
  };
  if (ft) {
    ask();
    static_for<56 - SPLIT>([&](auto J) {
      constexpr int i = SPLIT + decltype(J)::value;
      run_item(IC<i>{});
      if constexpr (i == SEND) { got(); }
      static_for<4>([&](auto C) {
        constexpr int j = decltype(C)::value;
        if constexpr (i == SEND + j) {
          if constexpr (j == HPOS) hint_st((unsigned)tagv, rh, hu_x);
          u32x4 o;
          _Pragma("unroll") for (int e = 0; e < 4; ++e)
              o[e] = __builtin_amdgcn_perm(__float_as_uint(acc2[0][4 * j + e] + sib[j][e]), (unsigned)(tag >> (32 * (j & 1))), 0x07060500u + e);
          st_sc1_at<j * 1024>(o, rh, vl16, sp_x);
          if constexpr (j == 3) __builtin_amdgcn_s_setprio(0);
          SB();
        }
      });
    });
    hand_over(acc2[1]);
    __builtin_amdgcn_s_waitcnt(0xC07F);
  } else {
    u64 prb; u32x4 pin_[4];
    static_for<57 - SPLIT>([&](auto J) {
      constexpr int i = SPLIT + decltype(J)::value;
      if constexpr (i == PRB[0]) probe<0>(prb, rh, hu_x);
      if constexpr (i == PRB[1]) probe<1>(prb, rh, hu_x);
      if constexpr (i < 56) run_item(IC<i>{});
      if constexpr (i == SEND) { hand_over(acc2[0]); __builtin_amdgcn_s_setprio(0); }
    });
    unsigned left = SPIN_CAP;
    fetch_when_told(left, prb, pin_, (unsigned)tag, vl16, rh, hu_x, sp_x);
    ask(); got();
    float s[16];
    _Pragma("unroll") for (int e = 0; e < 16; ++e) { s[e] = sib[e >> 2][e & 3] + acc2[1][e]; pin(s[e]); }
    left = SPIN_CAP;
    until_tagged(left, prb, pin_, tag, vl16, rh, sp_x);
    _Pragma("unroll") for (int j = 0; j < 2; ++j) {
      u32x4 o;
      _Pragma("unroll") for (int e = 0; e < 4; ++e)
          o[e] = pack2(s[8 * j + 2 * e] + __uint_as_float(pin_[2 * j + (e >> 1)][(2 * e) & 3] & 0xffffff00u), s[8 * j + 2 * e + 1] + __uint_as_float(pin_[2 * j + (e >> 1)][(2 * e + 1) & 3] & 0xffffff00u));
      *(u32x4*)(out + vout + 32 * j) = o;
    }
  }
  asm volatile("s_endpgm" ::: "memory");
}
}

extern "C" void kernel_launch(void* const* d_in, const int* in_sizes, int n_in, void* d_out, int out_size, void* d_ws, size_t ws_size,
                              hipStream_t stream) {
  ffn::ffn_kernel<<<256, 512, 0, stream>>>((const char*)d_in[0], (const char*)d_in[1], (const char*)d_in[2], (const char*)d_in[3],
                                           (const char*)d_in[4], (char*)d_out, (char*)d_ws);
}
```
